```python
import torch

def _softmax(x, axis=-1):
    return torch.softmax(x, dim=axis)
def _gelu(x):
    return torch.nn.functional.gelu(x, approximate="tanh")
def _silu(x):
    return torch.nn.functional.silu(x)
def _sigmoid(x):
    return torch.sigmoid(x)
def _layernorm(x, axis=-1, eps=1e-5):
    m = x.mean(dim=axis, keepdim=True)
    v = x.var(dim=axis, keepdim=True, unbiased=False)
    return (x - m) / torch.sqrt(v + eps)
def _scatter_add(tgt, idx, src, axis=0):
    return tgt.clone().index_add_(axis, idx, src)
def _segment_sum(src, seg, n):
    out = torch.zeros((n,) + src.shape[1:], dtype=src.dtype)
    return out.index_add_(0, seg, src)
def _pad(x, pw):
    flat = tuple(v for pair in reversed(pw) for v in pair)
    return torch.nn.functional.pad(x, flat)

kernel_name = 'concat_heads__gqa_block_cached__gqa_block_cached__near_ridge'

def setup_inputs():

    _w = (lambda a: (lambda t: t.to(torch.bfloat16) if t.is_floating_point() else t)(torch.as_tensor(a)))
    def _A_setup():
        B, Sq, D = 32, 32, 512
        Hq, Hkv, Sk, Dh = 32, 8, 512, 128
        assert Hq % Hkv == 0, (Hq, Hkv)
        return {
            "x": _w((lambda *s: torch.randn(*s, dtype=torch.bfloat16) * 0.8)(B, Sq, D)),
            "k_cache": _w((lambda *s: torch.randn(*s, dtype=torch.bfloat16) * 0.8)(B, Hkv, Sk, Dh)),
            "v_cache": _w((lambda *s: torch.randn(*s, dtype=torch.bfloat16) * 0.05)(B, Hkv, Sk, Dh)),
            "wq": _w((lambda *s: torch.randn(*s, dtype=torch.bfloat16) * 0.8)(D, Hq * Dh) * (D ** -0.5)),
            "wo": _w((lambda *s: torch.randn(*s, dtype=torch.bfloat16) * 0.05)(Hq * Dh, D) * ((Hq * Dh) ** -0.5)),
        }
    _A_in = _A_setup()

    def _B_setup():
        B, Sq, D = 32, 32, 512
        Hq, Hkv, Sk, Dh = 32, 8, 512, 128
        assert Hq % Hkv == 0, (Hq, Hkv)
        return {
            "x": _w((lambda *s: torch.randn(*s, dtype=torch.bfloat16) * 0.8)(B, Sq, D)),
            "k_cache": _w((lambda *s: torch.randn(*s, dtype=torch.bfloat16) * 0.8)(B, Hkv, Sk, Dh)),
            "v_cache": _w((lambda *s: torch.randn(*s, dtype=torch.bfloat16) * 0.05)(B, Hkv, Sk, Dh)),
            "wq": _w((lambda *s: torch.randn(*s, dtype=torch.bfloat16) * 0.8)(D, Hq * Dh) * (D ** -0.5)),
            "wo": _w((lambda *s: torch.randn(*s, dtype=torch.bfloat16) * 0.05)(Hq * Dh, D) * ((Hq * Dh) ** -0.5)),
        }
    _B_in = _B_setup()

    return {
        "A_x": _A_in["x"],
        "A_k_cache": _A_in["k_cache"],
        "A_v_cache": _A_in["v_cache"],
        "A_wq": _A_in["wq"],
        "A_wo": _A_in["wo"],
        "B_x": _B_in["x"],
        "B_k_cache": _B_in["k_cache"],
        "B_v_cache": _B_in["v_cache"],
        "B_wq": _B_in["wq"],
        "B_wo": _B_in["wo"],
    }

def reference(A_x, A_k_cache, A_v_cache, A_wq, A_wo, B_x, B_k_cache, B_v_cache, B_wq, B_wo):
    def _A_ref(x, k_cache, v_cache, wq, wo):
        B, Sq, D = x.shape
        Hq, Dh = 32, 128
        Hkv = k_cache.shape[1]
        rep = Hq // Hkv
        q = (lambda x, *d: x.permute(*d))((x @ wq).reshape(B, Sq, Hq, Dh), 0, 2, 1, 3)
        k = (lambda x, n, axis: torch.repeat_interleave(x, n, dim=axis))(k_cache, rep, axis=1)
        v = (lambda x, n, axis: torch.repeat_interleave(x, n, dim=axis))(v_cache, rep, axis=1)
        scores = torch.einsum("bhqd,bhkd->bhqk", q, k) * (Dh ** -0.5)
        p = _softmax(scores, axis=-1)
        o = torch.einsum("bhqk,bhkd->bhqd", p, v)
        return (lambda x, *d: x.permute(*d))(o, 0, 2, 1, 3).reshape(B, Sq, Hq * Dh) @ wo

    def _B_ref(x, k_cache, v_cache, wq, wo):
        B, Sq, D = x.shape
        Hq, Dh = 32, 128
        Hkv = k_cache.shape[1]
        rep = Hq // Hkv
        q = (lambda x, *d: x.permute(*d))((x @ wq).reshape(B, Sq, Hq, Dh), 0, 2, 1, 3)
        k = (lambda x, n, axis: torch.repeat_interleave(x, n, dim=axis))(k_cache, rep, axis=1)
        v = (lambda x, n, axis: torch.repeat_interleave(x, n, dim=axis))(v_cache, rep, axis=1)
        scores = torch.einsum("bhqd,bhkd->bhqk", q, k) * (Dh ** -0.5)
        p = _softmax(scores, axis=-1)
        o = torch.einsum("bhqk,bhkd->bhqd", p, v)
        return (lambda x, *d: x.permute(*d))(o, 0, 2, 1, 3).reshape(B, Sq, Hq * Dh) @ wo

    a = _A_ref(A_x, A_k_cache, A_v_cache, A_wq, A_wo)
    b = _B_ref(B_x, B_k_cache, B_v_cache, B_wq, B_wo)
    return torch.concatenate([a, b], axis=-1)
```

```cpp
#include <hip/hip_runtime.h>
#include <hip/hip_bf16.h>
typedef unsigned short u16;
__device__ __forceinline__ float bf2f(u16 x){ return __uint_as_float(((unsigned)x)<<16); }
__device__ __forceinline__ u16 f2bf(float f){ unsigned u=__float_as_uint(f); u += 0x7fffu + ((u>>16)&1u); return (u16)(u>>16); }
__global__ void gemm_naive(const u16* A, const u16* B, u16* C, int M, int N, int K, int ldc){
  int n = blockIdx.x*256+threadIdx.x; int m = blockIdx.y;
  float acc=0.f;
  for(int k=0;k<K;++k) acc += bf2f(A[(size_t)m*K+k])*bf2f(B[(size_t)k*N+n]);
  C[(size_t)m*ldc+n]=f2bf(acc);
}
__global__ void attn_naive(const u16* q, const u16* k, const u16* v, u16* o){
  int row = blockIdx.x;
  int h = row & 31, sq = (row>>5)&31, b = row>>10;
  int hk = h>>2;
  __shared__ float qs[128]; __shared__ float p[512]; __shared__ float red[512];
  int t = threadIdx.x;
  if(t<128) qs[t]=bf2f(q[((size_t)(b*32+sq))*4096 + h*128 + t]);
  __syncthreads();
  const u16* kr = k + (((size_t)b*8+hk)*512 + t)*128;
  float s=0; for(int d=0;d<128;++d) s+=qs[d]*bf2f(kr[d]);
  s *= 0.08838834764831845f;
  red[t]=s; __syncthreads();
  for(int o2=256;o2>0;o2>>=1){ if(t<o2) red[t]=fmaxf(red[t],red[t+o2]); __syncthreads(); }
  float m=red[0]; __syncthreads();
  float e=__expf(s-m); red[t]=e; __syncthreads();
  for(int o2=256;o2>0;o2>>=1){ if(t<o2) red[t]+=red[t+o2]; __syncthreads(); }
  float l=red[0];
  p[t]=e/l; __syncthreads();
  if(t<128){ float acc=0; const u16* vr = v + (((size_t)b*8+hk)*512)*128 + t; for(int j=0;j<512;++j) acc+=p[j]*bf2f(vr[(size_t)j*128]); o[((size_t)(b*32+sq))*4096 + h*128 + t]=f2bf(acc);}
}
extern "C" void kernel_launch(void* const* d_in, const int* in_sizes, int n_in, void* d_out, int out_size, void* d_ws, size_t ws_size, hipStream_t stream){
  u16* ws=(u16*)d_ws;
  for(int br=0;br<2;++br){
    const u16* x=(const u16*)d_in[br*5+0]; const u16* k=(const u16*)d_in[br*5+1]; const u16* v=(const u16*)d_in[br*5+2]; const u16* wq=(const u16*)d_in[br*5+3]; const u16* wo=(const u16*)d_in[br*5+4];
    u16* q=ws+ (size_t)br*2*1024*4096; u16* o=q+(size_t)1024*4096;
    gemm_naive<<<dim3(16,1024),256,0,stream>>>(x,wq,q,1024,4096,512,4096);
    attn_naive<<<32768,512,0,stream>>>(q,k,v,o);
    gemm_naive<<<dim3(2,1024),256,0,stream>>>(o,wo,(u16*)d_out+br*512,1024,512,4096,1024);
  }
}
```

```cpp
#include <hip/hip_runtime.h>
typedef unsigned u32;
typedef unsigned short u16;
typedef float f32x2 __attribute__((ext_vector_type(2)));
typedef float f32x4 __attribute__((ext_vector_type(4)));
typedef float f32x16 __attribute__((ext_vector_type(16)));
typedef __bf16 bf16x2 __attribute__((ext_vector_type(2)));
typedef __bf16 bf16x4 __attribute__((ext_vector_type(4)));
typedef __bf16 bf16x8 __attribute__((ext_vector_type(8)));
typedef u32 u32x2 __attribute__((ext_vector_type(2)));
typedef u32 u32x4 __attribute__((ext_vector_type(4)));
#define LAS __attribute__((address_space(3)))
#define DI __device__ __forceinline__
#define KVNT " nt"
#define KVFIRST(bp) 0
DI u32 cvtpk(float lo, float hi){ f32x2 v={lo,hi}; bf16x2 b=__builtin_convertvector(v,bf16x2); return __builtin_bit_cast(u32,b);}
DI u32 rfl(u32 x){ return __builtin_amdgcn_readfirstlane(x);}
DI bf16x8 asb(u32x4 x){ return __builtin_bit_cast(bf16x8,x);}
template<int N> DI void waitvm(){ asm volatile("s_waitcnt vmcnt(%0)"::"i"(N):"memory"); }
DI void bar(){ asm volatile("s_waitcnt lgkmcnt(0)\n\ts_barrier":::"memory"); }
DI void dma_nt(u32 m0v,u32 voff,__amdgpu_buffer_rsrc_t rs,u32 soff){
  asm volatile("s_mov_b32 m0, %0\n\ts_nop 0\n\tbuffer_load_dwordx4 %1, %2, %3 offen" KVNT " lds"::"s"(m0v),"v"(voff),"s"(rs),"s"(soff):"memory");
}
DI void dma(u32 m0v,u32 voff,__amdgpu_buffer_rsrc_t rs,u32 soff){
  asm volatile("s_mov_b32 m0, %0\n\ts_nop 0\n\tbuffer_load_dwordx4 %1, %2, %3 offen lds"::"s"(m0v),"v"(voff),"s"(rs),"s"(soff):"memory");
}
#define MF16(a,b,c) __builtin_amdgcn_mfma_f32_16x16x32_bf16((a),(b),(c),0,0,0)
#define MF32(a,b,c) __builtin_amdgcn_mfma_f32_32x32x16_bf16((a),(b),(c),0,0,0)
constexpr u32 XOFF=98304, OOFF=131072;
constexpr float QSCALE=0.12751743082459868f;
constexpr float THR=8.0f;

DI u32x4 afrag(const u32x4* raw,int t){
  u32x4 a;
  #pragma unroll
  for(int m=0;m<4;++m) a[m]=__builtin_amdgcn_perm(raw[2*m+1][t>>1],raw[2*m][t>>1],(t&1)?0x07060302u:0x05040100u);
  return a;
}

extern "C" __global__ __launch_bounds__(256,2) void fused_attn(const u16* xA,const u16* kA,const u16* vA,const u16* wqA,const u16* woA,
    const u16* xB,const u16* kB,const u16* vB,const u16* wqB,const u16* woB,u16* part){
  extern __shared__ __attribute__((aligned(16))) char shm[];
  const u32 tid=threadIdx.x, lane=tid&63, w=rfl(tid>>6);
  const u32 bid=blockIdx.x, hkv=bid&7, r=bid>>3, br=r&1, bp=r>>1;
  const u16* x=br?xB:xA; const u16* kk=br?kB:kA; const u16* vv=br?vB:vA; const u16* wq=br?wqB:wqA; const u16* wo=br?woB:woA;
  auto rx=__builtin_amdgcn_make_buffer_rsrc((void*)x,(short)0,1048576,0x00020000);
  auto rk=__builtin_amdgcn_make_buffer_rsrc((void*)kk,(short)0,33554432,0x00020000);
  auto rv=__builtin_amdgcn_make_buffer_rsrc((void*)vv,(short)0,33554432,0x00020000);
  auto rwq=__builtin_amdgcn_make_buffer_rsrc((void*)wq,(short)0,4194304,0x00020000);
  auto rwo=__builtin_amdgcn_make_buffer_rsrc((void*)wo,(short)0,4194304,0x00020000);
  auto rp=__builtin_amdgcn_make_buffer_rsrc((void*)part,(short)0,16777216,0x00020000);
  const u32 lb=(u32)(size_t)(LAS char*)shm;
  const u32 c=lane&15, g=lane>>4, l31=lane&31, h=lane>>5;
  const u32 vok=g*256+((c^(4*w+g))*16)+w*1024;
  const u32 vov=g*256+((c^(g<<2))*16)+w*1024;
  auto kvdma=[&](u32 n){
    u32 gb=(((2*bp+(n>>3))*8+hkv)*131072u)+(n&7)*16384u;
    u32 l0=lb+(n&3)*32768u+w*1024u;
    #pragma unroll
    for(int j=0;j<4;++j) dma_nt(l0+j*4096u,vok,rk,gb+j*4096u);
    #pragma unroll
    for(int j=0;j<4;++j) dma_nt(l0+16384u+j*4096u,vov,rv,gb+j*4096u);
  };
  const bool kvfirst=KVFIRST(bp);
  if(kvfirst){ kvdma(0);kvdma(1);kvdma(2);}
  #pragma unroll
  for(int i=0;i<16;++i) dma(lb+XOFF+(16*w+i)*1024u,(lane^i)*16u,rx,(64*bp+16*w+i)*1024u);
  u32x4 Q[2][8];
  {
    f32x4 acc[8][4];
    #pragma unroll
    for(int t=0;t<8;++t)
      #pragma unroll
      for(int tt=0;tt<4;++tt) acc[t][tt]=f32x4{0,0,0,0};
    const u32 vw=g*8*8192u+(hkv*512+w*128+c*8)*2u;
    u32x4 raw[2][8];
    auto ldw=[&](u32x4* d,u32 s){
      #pragma unroll
      for(int j=0;j<8;++j) d[j]=__builtin_amdgcn_raw_buffer_load_b128(rwq,vw,(s*32+j)*8192u,0);
    };
    ldw(raw[0],bp&15); ldw(raw[1],(bp+1)&15);
    waitvm<16>(); bar();
    const char* xb=shm+XOFF+c*1024;
    auto step=[&](u32x4* rw,u32 s,bool more,u32 snext){
      bf16x8 xf[4];
      #pragma unroll
      for(int tt=0;tt<4;++tt) xf[tt]=*(const bf16x8*)(xb+tt*16384+(((4*s+g)^c)*16));
      u32x4 a[8];
      #pragma unroll
      for(int t=0;t<8;++t) a[t]=afrag(rw,t);
      if(more) ldw(rw,snext);
      #pragma unroll
      for(int t=0;t<8;++t)
        #pragma unroll
        for(int tt=0;tt<4;++tt) acc[t][tt]=MF16(asb(a[t]),xf[tt],acc[t][tt]);
    };
    for(u32 i=0;i<14;i+=2){
      step(raw[0],(bp+i)&15,true,(bp+i+2)&15);
      step(raw[1],(bp+i+1)&15,true,(bp+i+3)&15);
    }
    step(raw[0],(bp+14)&15,false,0);
    step(raw[1],(bp+15)&15,false,0);
    if(!kvfirst){ asm volatile("":"+v"(acc[7][3])); kvdma(0);kvdma(1);kvdma(2);}
    #pragma unroll
    for(int u=0;u<2;++u)
      #pragma unroll
      for(int reg=0;reg<4;++reg)
        #pragma unroll
        for(int m=0;m<4;++m){
          u32 a0=cvtpk(acc[2*m][2*u][reg]*QSCALE,acc[2*m+1][2*u][reg]*QSCALE);
          u32 a1=cvtpk(acc[2*m][2*u+1][reg]*QSCALE,acc[2*m+1][2*u+1][reg]*QSCALE);
          auto sw=__builtin_amdgcn_permlane16_swap(a0,a1,false,false);
          Q[u][reg][m]=sw[0]; Q[u][4+reg][m]=sw[1];
        }
  }
  u32 kofs[8];
  #pragma unroll
  for(int i=0;i<8;++i) kofs[i]=l31*256+(((8*h+i)^(l31&15))*16);
  u32 vofs[4];
  { u32 q=(lane&15)>>2,p=lane&3,cb=(lane>>4)&1;
    #pragma unroll
    for(int db=0;db<4;++db) vofs[db]=16384+(4*h+q)*256+((4*(db^q)+2*cb+(p>>1))*16)+(p&1)*8; }
  const u32 vwo=(16*(g>>1)+4*(g&1))*1024u+(w*128+8*(4*(c&3)+(c>>2)))*2u;
  #pragma unroll
  for(int u=0;u<2;++u){
    f32x16 O[4];
    #pragma unroll
    for(int db=0;db<4;++db) O[db]=f32x16{};
    float negm=0.f,lsum=0.f;
    for(u32 t=0;t<8;++t){
      const u32 n=u*8+t;
      if(n<14) waitvm<16>(); else if(n==14) waitvm<8>(); else waitvm<0>();
      bar();
      if(n+3<16) kvdma(n+3);
      const char* sb=shm+(n&3)*32768;
      f32x16 S[2];
      #pragma unroll
      for(int kb=0;kb<2;++kb){
        #pragma unroll
        for(int e=0;e<16;++e) S[kb][e]=negm;
        #pragma unroll
        for(int i=0;i<8;++i){ bf16x8 kf=*(const bf16x8*)(sb+kb*8192+kofs[i]); S[kb]=MF32(kf,asb(Q[u][i]),S[kb]); }
      }
      float pm=S[0][0];
      #pragma unroll
      for(int e=1;e<16;++e) pm=fmaxf(pm,S[0][e]);
      #pragma unroll
      for(int e=0;e<16;++e) pm=fmaxf(pm,S[1][e]);
      if(t==0 || __builtin_expect(__any(pm>THR),0)){
        auto sw=__builtin_amdgcn_permlane32_swap(__float_as_uint(pm),__float_as_uint(pm),false,false);
        float d=fmaxf(__uint_as_float(sw[0]),__uint_as_float(sw[1]));
        if(t!=0) d=fmaxf(d,0.f);
        float f=__builtin_amdgcn_exp2f(-d);
        if(t!=0){
          #pragma unroll
          for(int db=0;db<4;++db)
            #pragma unroll
            for(int e=0;e<16;++e) O[db][e]*=f;
          lsum*=f;
        }
        negm-=d;
        #pragma unroll
        for(int kb=0;kb<2;++kb)
          #pragma unroll
          for(int e=0;e<16;++e) S[kb][e]-=d;
      }
      u32x4 P[2][2];
      #pragma unroll
      for(int kb=0;kb<2;++kb){
        #pragma unroll
        for(int e=0;e<16;++e){ S[kb][e]=__builtin_amdgcn_exp2f(S[kb][e]); lsum+=S[kb][e]; }
        #pragma unroll
        for(int s=0;s<2;++s)
          #pragma unroll
          for(int m=0;m<4;++m) P[kb][s][m]=cvtpk(S[kb][8*s+2*m],S[kb][8*s+2*m+1]);
      }
      #pragma unroll
      for(int kb=0;kb<2;++kb)
        #pragma unroll
        for(int s=0;s<2;++s)
          #pragma unroll
          for(int db=0;db<4;++db){
            bf16x4 lo=__builtin_amdgcn_ds_read_tr16_b64_v4bf16((LAS bf16x4*)(sb+vofs[db]+(32*kb+16*s)*256));
            bf16x4 hi=__builtin_amdgcn_ds_read_tr16_b64_v4bf16((LAS bf16x4*)(sb+vofs[db]+(32*kb+16*s+8)*256));
            bf16x8 vf=__builtin_shufflevector(lo,hi,0,1,2,3,4,5,6,7);
            O[db]=MF32(vf,asb(P[kb][s]),O[db]);
          }
    }
    u32x4 raw[2][8];
    auto ldw=[&](u32x4* d,u32 s){
      #pragma unroll
      for(int j=0;j<8;++j) d[j]=__builtin_amdgcn_raw_buffer_load_b128(rwo,vwo+(j>>2)*8192u+(j&3)*1024u,hkv*524288u+s*32768u,0);
    };
    ldw(raw[0],bp&15); ldw(raw[1],(bp+1)&15);
    {
      auto sw=__builtin_amdgcn_permlane32_swap(__float_as_uint(lsum),__float_as_uint(lsum),false,false);
      float inv=1.0f/(__uint_as_float(sw[0])+__uint_as_float(sw[1]));
      #pragma unroll
      for(int db=0;db<4;++db)
        #pragma unroll
        for(int qp=0;qp<2;++qp){
          u32x4 v;
          #pragma unroll
          for(int m=0;m<4;++m) v[m]=cvtpk(O[db][8*qp+2*m]*inv,O[db][8*qp+2*m+1]*inv);
          *(u32x4*)(shm+OOFF+l31*1024+(((16*w+4*db+2*qp+h)^(l31&15))*16))=v;
        }
    }
    bar();
    f32x4 acc[8][2];
    #pragma unroll
    for(int t=0;t<8;++t){acc[t][0]=f32x4{0,0,0,0};acc[t][1]=f32x4{0,0,0,0};}
    const char* ob=shm+OOFF+c*1024;
    auto step=[&](u32x4* rw,u32 s,bool more,u32 snext){
      bf16x8 of[2];
      #pragma unroll
      for(int tt=0;tt<2;++tt) of[tt]=*(const bf16x8*)(ob+tt*16384+(((4*s+g)^c)*16));
      u32x4 a[8];
      #pragma unroll
      for(int t=0;t<8;++t) a[t]=afrag(rw,t);
      if(more) ldw(rw,snext);
      #pragma unroll
      for(int t=0;t<8;++t)
        #pragma unroll
        for(int tt=0;tt<2;++tt) acc[t][tt]=MF16(asb(a[t]),of[tt],acc[t][tt]);
    };
    for(u32 i=0;i<14;i+=2){
      step(raw[0],(bp+i)&15,true,(bp+i+2)&15);
      step(raw[1],(bp+i+1)&15,true,(bp+i+3)&15);
    }
    step(raw[0],(bp+14)&15,false,0);
    step(raw[1],(bp+15)&15,false,0);
    #pragma unroll
    for(int tt=0;tt<2;++tt)
      #pragma unroll
      for(int reg=0;reg<4;++reg){
        u32x4 v;
        #pragma unroll
        for(int m=0;m<4;++m) v[m]=cvtpk(acc[2*m][tt][reg],acc[2*m+1][tt][reg]);
        __builtin_amdgcn_raw_buffer_store_b128(v,rp,lane*16u,((((r*2+u)*8+(2*w+tt))*8+hkv)*4+reg)*1024u,0);
      }
  }
}

extern "C" __global__ __launch_bounds__(256) void reduce_k(const u32x4* part,u16* out){
  const u32 id=blockIdx.x, j=id&7, u=(id>>3)&1, r=id>>4, br=r&1,bp=r>>1;
  const u32 lane=threadIdx.x&63, v=threadIdx.x>>6, c=lane&15,g=lane>>4;
  float s[8]={0,0,0,0,0,0,0,0};
  #pragma unroll
  for(int src=0;src<8;++src){
    u32x4 p=part[((id*8+src)*4+v)*64+lane];
    #pragma unroll
    for(int m=0;m<4;++m){ s[2*m]+=__uint_as_float(p[m]<<16); s[2*m+1]+=__uint_as_float(p[m]&0xffff0000u); }
  }
  u32x4 o;
  #pragma unroll
  for(int m=0;m<4;++m) o[m]=cvtpk(s[2*m],s[2*m+1]);
  const u32 tok=(2*bp+u)*32+16*(j&1)+c;
  *(u32x4*)(out+(size_t)tok*1024+br*512+128*(j>>1)+32*v+8*g)=o;
}

extern "C" void kernel_launch(void* const* d_in, const int* in_sizes, int n_in, void* d_out, int out_size, void* d_ws, size_t ws_size, hipStream_t stream){
  (void)hipFuncSetAttribute((const void*)fused_attn,hipFuncAttributeMaxDynamicSharedMemorySize,163840);
  fused_attn<<<256,256,163840,stream>>>((const u16*)d_in[0],(const u16*)d_in[1],(const u16*)d_in[2],(const u16*)d_in[3],(const u16*)d_in[4],
     (const u16*)d_in[5],(const u16*)d_in[6],(const u16*)d_in[7],(const u16*)d_in[8],(const u16*)d_in[9],(u16*)d_ws);
  reduce_k<<<512,256,0,stream>>>((const u32x4*)d_ws,(u16*)d_out);
}
```

```cpp
#include <hip/hip_runtime.h>

extern "C" __global__ __launch_bounds__(256, 1) void gqa_block_fused(
    const void* xA, const void* kA, const void* vA, const void* wqA, const void* woA,
    const void* xB, const void* kB, const void* vB, const void* wqB, const void* woB,
    void* out, void* ws) {
  const unsigned bid = blockIdx.x;
  const unsigned w = __builtin_amdgcn_readfirstlane(threadIdx.x >> 6);
  const void* ka = (const void*)__builtin_amdgcn_kernarg_segment_ptr();
  asm volatile(
    "s_nop 4\n"
    "v_mbcnt_lo_u32_b32 v0, -1, 0\n"
    "v_mbcnt_hi_u32_b32 v0, -1, v0\n"
    "s_lshr_b32 s66, %7, 2\n"
    "s_xor_b32 s66, s66, %7\n"
    "s_and_b32 s66, s66, 1\n"
    "s_mul_i32 s78, s66, 40\n"
    "s_load_dwordx2 s[56:57], %0, 0x58\n"
    "s_load_dwordx2 s[60:61], %0, 0x50\n"
    "s_mov_b64 s[80:81], %0\n"
    "s_add_u32 s80, s80, s78\n"
    "s_addc_u32 s81, s81, 0\n"
    "s_load_dwordx2 s[48:49], s[80:81], 0x18\n"
    "s_load_dwordx2 s[36:37], s[80:81], 0x0\n"
    "s_load_dwordx2 s[40:41], s[80:81], 0x8\n"
    "s_load_dwordx2 s[44:45], s[80:81], 0x10\n"
    "s_load_dwordx2 s[52:53], s[80:81], 0x20\n"
    "s_getpc_b64 s[72:73]\n"
    "Lpc_%=:\n"
    "s_and_b32 s64, %7, 6\n"
    "s_bfe_u32 s78, %7, 65539\n"
    "s_add_u32 s64, s64, s78\n"
    "s_lshr_b32 s65, %7, 4\n"
    "s_lshl_b32 s67, s66, 4\n"
    "s_add_u32 s67, s67, s65\n"
    "s_mov_b32 s68, %8\n"
    "s_bitcmp1_b32 %7, 2\n"
    "s_mov_b32 s77, 556016164\n"
    "s_cselect_b32 s77, 488643357, s77\n"
    "s_and_b32 s79, %7, 3\n"
    "s_lshl_b32 s79, s79, 3\n"
    "s_lshr_b32 s77, s77, s79\n"
    "s_and_b32 s77, s77, 0xff\n"
    "s_sub_u32 s77, s77, 32\n"
    "v_and_b32 v4, 15, v0\n"
    "v_lshrrev_b32 v5, 4, v0\n"
    "s_lshl_b32 s78, s64, 10\n"
    "s_lshl_b32 s79, s68, 8\n"
    "s_add_u32 s78, s78, s79\n"
    "v_lshlrev_b32 v48, 16, v5\n"
    "v_lshl_add_u32 v48, v4, 4, v48\n"
    "v_add_u32 v48, s78, v48\n"
    "s_lshl_b32 s85, s65, 18\n"
    "s_mov_b32 s90, 262144\n"
    "s_mov_b32 s91, 0x3fffff\n"
    "s_and_b32 s73, s73, 0xffff\n"
    "s_mov_b32 s74, Lrxa_%=-Lpc_%=\n"
    "s_mov_b32 s75, 0x20000\n"
    "v_lshl_add_u32 v2, s68, 6, v0\n"
    "v_cmp_gt_u32 s[98:99], 8, v2\n"
    "v_lshlrev_b32 v1, 7, v0\n"
    "v_bfrev_b32 v2, 1\n"
    "v_cndmask_b32 v1, v2, v1, s[98:99]\n"
    "s_lshr_b32 s78, %7, 3\n"
    "s_lshl_b32 s78, s78, 10\n"
    "buffer_load_dword v31, v1, s[72:75], s78 offen\n"
    "v_add_u32 v49, 8192, v48\n"
    "v_add_u32 v50, 16384, v48\n"
    "v_add_u32 v51, 24576, v48\n"
    "v_add_u32 v52, 32768, v48\n"
    "v_add_u32 v53, 40960, v48\n"
    "v_add_u32 v54, 49152, v48\n"
    "v_add_u32 v55, 57344, v48\n"
    "s_waitcnt lgkmcnt(0)\n"
    "s_and_b32 s49, s49, 0xffff\n"
    "s_mov_b32 s50, 4194304\n"
    "s_mov_b32 s51, 0x20000\n"
    "buffer_load_dwordx4 v[128:131], v48, s[48:51], s85 offen\n"
    "buffer_load_dwordx4 v[132:135], v49, s[48:51], s85 offen\n"
    "buffer_load_dwordx4 v[136:139], v50, s[48:51], s85 offen\n"
    "buffer_load_dwordx4 v[140:143], v51, s[48:51], s85 offen\n"
    "buffer_load_dwordx4 v[144:147], v52, s[48:51], s85 offen\n"
    "buffer_load_dwordx4 v[148:151], v53, s[48:51], s85 offen\n"
    "buffer_load_dwordx4 v[152:155], v54, s[48:51], s85 offen\n"
    "buffer_load_dwordx4 v[156:159], v55, s[48:51], s85 offen\n"
    "s_and_b32 s57, s57, 0xffff\n"
    "s_mov_b32 s58, 18874368\n"
    "s_mov_b32 s59, 0x20000\n"
    "s_add_u32 s85, s85, s90\n"
    "s_and_b32 s85, s85, s91\n"
    "s_lshl_b32 s97, s67, 3\n"
    "s_add_u32 s97, s97, s64\n"
    "s_lshl_b32 s97, s97, 7\n"
    "s_add_u32 s97, s97, 16777216\n"
    "v_lshlrev_b32 v1, 2, v0\n"
    "buffer_load_dword v3, v1, s[56:59], s97 offen sc1\n"
    "s_and_b32 s37, s37, 0xffff\n"
    "s_mov_b32 s38, 1048576\n"
    "s_mov_b32 s39, 0x20000\n"
    "s_and_b32 s41, s41, 0xffff\n"
    "s_mov_b32 s42, 33554432\n"
    "s_mov_b32 s43, 0x20000\n"
    "s_and_b32 s45, s45, 0xffff\n"
    "s_mov_b32 s46, 33554432\n"
    "s_mov_b32 s47, 0x20000\n"
    "s_and_b32 s53, s53, 0xffff\n"
    "s_mov_b32 s54, 4194304\n"
    "s_mov_b32 s55, 0x20000\n"
    "s_and_b32 s61, s61, 0xffff\n"
    "s_mov_b32 s62, 2097152\n"
    "s_mov_b32 s63, 0x20000\n"
    "s_lshl_b32 s78, s65, 16\n"
    "s_lshl_b32 s79, s68, 14\n"
    "s_add_u32 s78, s78, s79\n"
    "s_add_u32 s79, s79, 98304\n"
    "s_bfe_u32 s95, s64, 65536\n"
    "s_lshl_b32 s95, s95, 3\n"
    "v_xor_b32 v29, s95, v0\n"
    "s_lshl_b32 s95, s95, 10\n"
    "s_xor_b32 s78, s78, s95\n"
    "s_xor_b32 s79, s79, s95\n"
    "v_xor_b32 v2, 0, v29\n"
    "v_lshlrev_b32 v2, 4, v2\n"
    "s_xor_b32 m0, s79, 0\n"
    "s_xor_b32 s94, s78, 0\n"
    "buffer_load_dwordx4 v2, s[36:39], s94 offen offset:0 lds\n"
    "v_xor_b32 v1, 1, v29\n"
    "v_lshlrev_b32 v1, 4, v1\n"
    "buffer_load_dwordx4 v1, s[36:39], s94 offen offset:1024 lds\n"
    "v_xor_b32 v2, 2, v29\n"
    "v_lshlrev_b32 v2, 4, v2\n"
    "buffer_load_dwordx4 v2, s[36:39], s94 offen offset:2048 lds\n"
    "v_xor_b32 v1, 3, v29\n"
    "v_lshlrev_b32 v1, 4, v1\n"
    "buffer_load_dwordx4 v1, s[36:39], s94 offen offset:3072 lds\n"
    "v_xor_b32 v2, 4, v29\n"
    "v_lshlrev_b32 v2, 4, v2\n"
    "s_xor_b32 m0, s79, 4096\n"
    "s_xor_b32 s94, s78, 4096\n"
    "buffer_load_dwordx4 v2, s[36:39], s94 offen offset:0 lds\n"
    "v_xor_b32 v1, 5, v29\n"
    "v_lshlrev_b32 v1, 4, v1\n"
    "buffer_load_dwordx4 v1, s[36:39], s94 offen offset:1024 lds\n"
    "v_xor_b32 v2, 6, v29\n"
    "v_lshlrev_b32 v2, 4, v2\n"
    "buffer_load_dwordx4 v2, s[36:39], s94 offen offset:2048 lds\n"
    "v_xor_b32 v1, 7, v29\n"
    "v_lshlrev_b32 v1, 4, v1\n"
    "buffer_load_dwordx4 v1, s[36:39], s94 offen offset:3072 lds\n"
    "v_xor_b32 v2, 8, v29\n"
    "v_lshlrev_b32 v2, 4, v2\n"
    "s_xor_b32 m0, s79, 8192\n"
    "s_xor_b32 s94, s78, 8192\n"
    "buffer_load_dwordx4 v2, s[36:39], s94 offen offset:0 lds\n"
    "v_xor_b32 v1, 9, v29\n"
    "v_lshlrev_b32 v1, 4, v1\n"
    "buffer_load_dwordx4 v1, s[36:39], s94 offen offset:1024 lds\n"
    "v_xor_b32 v2, 10, v29\n"
    "v_lshlrev_b32 v2, 4, v2\n"
    "buffer_load_dwordx4 v2, s[36:39], s94 offen offset:2048 lds\n"
    "v_xor_b32 v1, 11, v29\n"
    "v_lshlrev_b32 v1, 4, v1\n"
    "buffer_load_dwordx4 v1, s[36:39], s94 offen offset:3072 lds\n"
    "v_xor_b32 v2, 12, v29\n"
    "v_lshlrev_b32 v2, 4, v2\n"
    "s_xor_b32 m0, s79, 12288\n"
    "s_xor_b32 s94, s78, 12288\n"
    "buffer_load_dwordx4 v2, s[36:39], s94 offen offset:0 lds\n"
    "v_xor_b32 v1, 13, v29\n"
    "v_lshlrev_b32 v1, 4, v1\n"
    "buffer_load_dwordx4 v1, s[36:39], s94 offen offset:1024 lds\n"
    "v_xor_b32 v2, 14, v29\n"
    "v_lshlrev_b32 v2, 4, v2\n"
    "buffer_load_dwordx4 v2, s[36:39], s94 offen offset:2048 lds\n"
    "v_xor_b32 v1, 15, v29\n"
    "v_lshlrev_b32 v1, 4, v1\n"
    "buffer_load_dwordx4 v1, s[36:39], s94 offen offset:3072 lds\n"
    "v_add_u32 v1, 0, v5\n"
    "v_xor_b32 v1, v1, v4\n"
    "v_lshlrev_b32 v1, 4, v1\n"
    "v_lshl_add_u32 v20, v5, 8, v1\n"
    "v_add_u32 v1, 4, v5\n"
    "v_xor_b32 v1, v1, v4\n"
    "v_lshlrev_b32 v1, 4, v1\n"
    "v_lshl_add_u32 v21, v5, 8, v1\n"
    "v_add_u32 v1, 8, v5\n"
    "v_xor_b32 v1, v1, v4\n"
    "v_lshlrev_b32 v1, 4, v1\n"
    "v_lshl_add_u32 v22, v5, 8, v1\n"
    "v_add_u32 v1, 12, v5\n"
    "v_xor_b32 v1, v1, v4\n"
    "v_lshlrev_b32 v1, 4, v1\n"
    "v_lshl_add_u32 v23, v5, 8, v1\n"
    "v_lshlrev_b32 v1, 2, v5\n"
    "v_xor_b32 v1, v1, v4\n"
    "v_lshlrev_b32 v1, 4, v1\n"
    "v_lshl_add_u32 v24, v5, 8, v1\n"
    "s_lshl_b32 s89, s65, 4\n"
    "s_add_u32 s89, s89, s64\n"
    "s_lshl_b32 s89, s89, 17\n"
    "s_lshl_b32 s78, s68, 12\n"
    "s_add_u32 s89, s89, s78\n"
    "s_add_u32 s35, s65, s64\n"
    "s_lshl_b32 s35, s35, 14\n"
    "s_and_b32 s35, s35, 0x1c000\n"
    "s_mov_b32 s92, s78\n"
    "s_add_u32 s93, s78, 65536\n"
    "s_bitcmp1_b32 s65, 0\n"
    "s_mov_b32 s71, 20752\n"
    "s_mov_b32 s70, 128880\n"
    "s_cselect_b32 s71, 17476, s71\n"
    "s_cselect_b32 s70, 122332, s70\n"
    "s_mov_b32 s84, 0\n"
    "v_mov_b32 v62, 0x05040100\n"
    "v_mov_b32 v63, 0x07060302\n"
    "s_mov_b32 s74, 0x3e0293ee\n"
    "s_mov_b32 s75, 0x3e0293ee\n"
    "s_mov_b32 s76, 0x53800000\n"
    "s_mov_b32 s69, 0\n"
    "s_mov_b32 s87, 0\n"
    "s_mov_b32 s83, 0\n"
    "v_xor_b32 v1, v4, v5\n"
    "v_lshlrev_b32 v1, 4, v1\n"
    "v_lshl_add_u32 v7, v4, 10, v1\n"
    "v_add_u32 v7, 98304, v7\n"
    "v_add_u32 v6, 32768, v7\n"
    "v_and_b32 v1, 31, v0\n"
    "v_lshrrev_b32 v2, 5, v0\n"
    "v_lshlrev_b32 v26, 3, v2\n"
    "v_xor_b32 v26, v26, v4\n"
    "v_lshlrev_b32 v26, 4, v26\n"
    "v_lshl_add_u32 v8, v1, 8, v26\n"
    "v_xor_b32 v9, 16, v8\n"
    "v_xor_b32 v10, 32, v8\n"
    "v_xor_b32 v11, 48, v8\n"
    "v_xor_b32 v12, 64, v8\n"
    "v_xor_b32 v13, 80, v8\n"
    "v_xor_b32 v14, 96, v8\n"
    "v_xor_b32 v15, 112, v8\n"
    "v_lshrrev_b32 v26, 2, v4\n"
    "v_lshl_add_u32 v28, v2, 2, v26\n"
    "v_lshlrev_b32 v28, 8, v28\n"
    "v_lshl_add_u32 v28, v26, 6, v28\n"
    "v_and_b32 v29, 1, v5\n"
    "v_lshl_add_u32 v28, v29, 5, v28\n"
    "v_and_b32 v29, 3, v0\n"
    "v_lshl_add_u32 v28, v29, 3, v28\n"
    "v_add_u32 v16, 65536, v28\n"
    "v_xor_b32 v17, 64, v16\n"
    "v_xor_b32 v18, 128, v16\n"
    "v_xor_b32 v19, 192, v16\n"
    "v_mov_b32 v64, 0\n"
    "v_mov_b32 v65, 0\n"
    "v_mov_b32 v66, 0\n"
    "v_mov_b32 v67, 0\n"
    "v_mov_b32 v68, 0\n"
    "v_mov_b32 v69, 0\n"
    "v_mov_b32 v70, 0\n"
    "v_mov_b32 v71, 0\n"
    "v_mov_b32 v72, 0\n"
    "v_mov_b32 v73, 0\n"
    "v_mov_b32 v74, 0\n"
    "v_mov_b32 v75, 0\n"
    "v_mov_b32 v76, 0\n"
    "v_mov_b32 v77, 0\n"
    "v_mov_b32 v78, 0\n"
    "v_mov_b32 v79, 0\n"
    "v_mov_b32 v80, 0\n"
    "v_mov_b32 v81, 0\n"
    "v_mov_b32 v82, 0\n"
    "v_mov_b32 v83, 0\n"
    "v_mov_b32 v84, 0\n"
    "v_mov_b32 v85, 0\n"
    "v_mov_b32 v86, 0\n"
    "v_mov_b32 v87, 0\n"
    "v_mov_b32 v88, 0\n"
    "v_mov_b32 v89, 0\n"
    "v_mov_b32 v90, 0\n"
    "v_mov_b32 v91, 0\n"
    "v_mov_b32 v92, 0\n"
    "v_mov_b32 v93, 0\n"
    "v_mov_b32 v94, 0\n"
    "v_mov_b32 v95, 0\n"
    "v_mov_b32 v96, 0\n"
    "v_mov_b32 v97, 0\n"
    "v_mov_b32 v98, 0\n"
    "v_mov_b32 v99, 0\n"
    "v_mov_b32 v100, 0\n"
    "v_mov_b32 v101, 0\n"
    "v_mov_b32 v102, 0\n"
    "v_mov_b32 v103, 0\n"
    "v_mov_b32 v104, 0\n"
    "v_mov_b32 v105, 0\n"
    "v_mov_b32 v106, 0\n"
    "v_mov_b32 v107, 0\n"
    "v_mov_b32 v108, 0\n"
    "v_mov_b32 v109, 0\n"
    "v_mov_b32 v110, 0\n"
    "v_mov_b32 v111, 0\n"
    "v_mov_b32 v112, 0\n"
    "v_mov_b32 v113, 0\n"
    "v_mov_b32 v114, 0\n"
    "v_mov_b32 v115, 0\n"
    "v_mov_b32 v116, 0\n"
    "v_mov_b32 v117, 0\n"
    "v_mov_b32 v118, 0\n"
    "v_mov_b32 v119, 0\n"
    "v_mov_b32 v120, 0\n"
    "v_mov_b32 v121, 0\n"
    "v_mov_b32 v122, 0\n"
    "v_mov_b32 v123, 0\n"
    "v_mov_b32 v124, 0\n"
    "v_mov_b32 v125, 0\n"
    "v_mov_b32 v126, 0\n"
    "v_mov_b32 v127, 0\n"
    "v_accvgpr_write_b32 a128, 0\n"
    "v_accvgpr_write_b32 a129, 0\n"
    "v_accvgpr_write_b32 a130, 0\n"
    "v_accvgpr_write_b32 a131, 0\n"
    "v_accvgpr_write_b32 a132, 0\n"
    "v_accvgpr_write_b32 a133, 0\n"
    "v_accvgpr_write_b32 a134, 0\n"
    "v_accvgpr_write_b32 a135, 0\n"
    "v_accvgpr_write_b32 a136, 0\n"
    "v_accvgpr_write_b32 a137, 0\n"
    "v_accvgpr_write_b32 a138, 0\n"
    "v_accvgpr_write_b32 a139, 0\n"
    "v_accvgpr_write_b32 a140, 0\n"
    "v_accvgpr_write_b32 a141, 0\n"
    "v_accvgpr_write_b32 a142, 0\n"
    "v_accvgpr_write_b32 a143, 0\n"
    "v_accvgpr_write_b32 a144, 0\n"
    "v_accvgpr_write_b32 a145, 0\n"
    "v_accvgpr_write_b32 a146, 0\n"
    "v_accvgpr_write_b32 a147, 0\n"
    "v_accvgpr_write_b32 a148, 0\n"
    "v_accvgpr_write_b32 a149, 0\n"
    "v_accvgpr_write_b32 a150, 0\n"
    "v_accvgpr_write_b32 a151, 0\n"
    "v_accvgpr_write_b32 a152, 0\n"
    "v_accvgpr_write_b32 a153, 0\n"
    "v_accvgpr_write_b32 a154, 0\n"
    "v_accvgpr_write_b32 a155, 0\n"
    "v_accvgpr_write_b32 a156, 0\n"
    "v_accvgpr_write_b32 a157, 0\n"
    "v_accvgpr_write_b32 a158, 0\n"
    "v_accvgpr_write_b32 a159, 0\n"
    "v_accvgpr_write_b32 a160, 0\n"
    "v_accvgpr_write_b32 a161, 0\n"
    "v_accvgpr_write_b32 a162, 0\n"
    "v_accvgpr_write_b32 a163, 0\n"
    "v_accvgpr_write_b32 a164, 0\n"
    "v_accvgpr_write_b32 a165, 0\n"
    "v_accvgpr_write_b32 a166, 0\n"
    "v_accvgpr_write_b32 a167, 0\n"
    "v_accvgpr_write_b32 a168, 0\n"
    "v_accvgpr_write_b32 a169, 0\n"
    "v_accvgpr_write_b32 a170, 0\n"
    "v_accvgpr_write_b32 a171, 0\n"
    "v_accvgpr_write_b32 a172, 0\n"
    "v_accvgpr_write_b32 a173, 0\n"
    "v_accvgpr_write_b32 a174, 0\n"
    "v_accvgpr_write_b32 a175, 0\n"
    "v_accvgpr_write_b32 a176, 0\n"
    "v_accvgpr_write_b32 a177, 0\n"
    "v_accvgpr_write_b32 a178, 0\n"
    "v_accvgpr_write_b32 a179, 0\n"
    "v_accvgpr_write_b32 a180, 0\n"
    "v_accvgpr_write_b32 a181, 0\n"
    "v_accvgpr_write_b32 a182, 0\n"
    "v_accvgpr_write_b32 a183, 0\n"
    "v_accvgpr_write_b32 a184, 0\n"
    "v_accvgpr_write_b32 a185, 0\n"
    "v_accvgpr_write_b32 a186, 0\n"
    "v_accvgpr_write_b32 a187, 0\n"
    "v_accvgpr_write_b32 a188, 0\n"
    "v_accvgpr_write_b32 a189, 0\n"
    "v_accvgpr_write_b32 a190, 0\n"
    "v_accvgpr_write_b32 a191, 0\n"
    "s_waitcnt vmcnt(16)\n"
    "s_mov_b32 s78, 0\n"
    "v_readlane_b32 s79, v3, 0\n"
    "s_nop 0\n"
    "s_bitset1_b32 s78, s79\n"
    "v_readlane_b32 s79, v3, 1\n"
    "s_nop 0\n"
    "s_bitset1_b32 s78, s79\n"
    "v_readlane_b32 s79, v3, 2\n"
    "s_nop 0\n"
    "s_bitset1_b32 s78, s79\n"
    "v_readlane_b32 s79, v3, 3\n"
    "s_nop 0\n"
    "s_bitset1_b32 s78, s79\n"
    "v_readlane_b32 s79, v3, 4\n"
    "s_nop 0\n"
    "s_bitset1_b32 s78, s79\n"
    "v_readlane_b32 s79, v3, 5\n"
    "s_nop 0\n"
    "s_bitset1_b32 s78, s79\n"
    "v_readlane_b32 s79, v3, 6\n"
    "s_nop 0\n"
    "s_bitset1_b32 s78, s79\n"
    "v_readlane_b32 s79, v3, 7\n"
    "s_nop 0\n"
    "s_bitset1_b32 s78, s79\n"
    "v_readlane_b32 s79, v3, 8\n"
    "s_nop 0\n"
    "s_bitset1_b32 s78, s79\n"
    "v_readlane_b32 s79, v3, 9\n"
    "s_nop 0\n"
    "s_bitset1_b32 s78, s79\n"
    "v_readlane_b32 s79, v3, 10\n"
    "s_nop 0\n"
    "s_bitset1_b32 s78, s79\n"
    "v_readlane_b32 s79, v3, 11\n"
    "s_nop 0\n"
    "s_bitset1_b32 s78, s79\n"
    "v_readlane_b32 s79, v3, 12\n"
    "s_nop 0\n"
    "s_bitset1_b32 s78, s79\n"
    "v_readlane_b32 s79, v3, 13\n"
    "s_nop 0\n"
    "s_bitset1_b32 s78, s79\n"
    "v_readlane_b32 s79, v3, 14\n"
    "s_nop 0\n"
    "s_bitset1_b32 s78, s79\n"
    "v_readlane_b32 s79, v3, 15\n"
    "s_nop 0\n"
    "s_bitset1_b32 s78, s79\n"
    "v_readlane_b32 s79, v3, 16\n"
    "s_nop 0\n"
    "s_bitset1_b32 s78, s79\n"
    "s_ff0_i32_b32 s78, s78\n"
    "v_mov_b32 v1, s78\n"
    "v_lshl_add_u32 v2, s68, 6, v0\n"
    "v_cmp_eq_u32 vcc, 0, v2\n"
    "v_bfrev_b32 v2, 1\n"
    "s_nop 0\n"
    "v_cndmask_b32 v2, v2, 64, vcc\n"
    "buffer_store_dword v1, v2, s[56:59], s97 offen sc1\n"
    "v_perm_b32 v32, v132, v128, v62\n"
    "v_perm_b32 v33, v140, v136, v62\n"
    "v_perm_b32 v34, v148, v144, v62\n"
    "v_perm_b32 v35, v156, v152, v62\n"
    "v_perm_b32 v36, v132, v128, v63\n"
    "buffer_load_dwordx4 v[160:163], v48, s[48:51], s85 offen\n"
    "buffer_load_dwordx4 v[164:167], v49, s[48:51], s85 offen\n"
    "buffer_load_dwordx4 v[168:171], v50, s[48:51], s85 offen\n"
    "buffer_load_dwordx4 v[172:175], v51, s[48:51], s85 offen\n"
    "buffer_load_dwordx4 v[176:179], v52, s[48:51], s85 offen\n"
    "buffer_load_dwordx4 v[180:183], v53, s[48:51], s85 offen\n"
    "buffer_load_dwordx4 v[184:187], v54, s[48:51], s85 offen\n"
    "buffer_load_dwordx4 v[188:191], v55, s[48:51], s85 offen\n"
    "s_add_u32 s85, s85, s90\n"
    "s_and_b32 s85, s85, s91\n"
    "buffer_load_dwordx4 v[192:195], v48, s[48:51], s85 offen\n"
    "buffer_load_dwordx4 v[196:199], v49, s[48:51], s85 offen\n"
    "buffer_load_dwordx4 v[200:203], v50, s[48:51], s85 offen\n"
    "buffer_load_dwordx4 v[204:207], v51, s[48:51], s85 offen\n"
    "buffer_load_dwordx4 v[208:211], v52, s[48:51], s85 offen\n"
    "buffer_load_dwordx4 v[212:215], v53, s[48:51], s85 offen\n"
    "buffer_load_dwordx4 v[216:219], v54, s[48:51], s85 offen\n"
    "buffer_load_dwordx4 v[220:223], v55, s[48:51], s85 offen\n"
    "s_add_u32 s85, s85, s90\n"
    "s_and_b32 s85, s85, s91\n"
    "s_lshl_b32 s86, s65, 6\n"
    "v_xor_b32 v1, s86, v7\n"
    "v_xor_b32 v2, s86, v6\n"
    "s_add_u32 s86, s86, 64\n"
    "s_and_b32 s86, s86, 0x3c0\n"
    "s_waitcnt vmcnt(17)\n"
    "s_barrier\n"
    "ds_read_b128 a[0:3], v1 offset:0\n"
    "ds_read_b128 a[4:7], v1 offset:16384\n"
    "ds_read_b128 a[8:11], v2 offset:0\n"
    "ds_read_b128 a[12:15], v2 offset:16384\n"
    "s_waitcnt lgkmcnt(0)\n"
    "Lq_%=:\n"
    "v_mfma_f32_16x16x32_bf16 v[64:67], v[32:35], a[0:3], v[64:67]\n"
    "v_perm_b32 v37, v140, v136, v63\n"
    "v_mfma_f32_16x16x32_bf16 v[68:71], v[32:35], a[4:7], v[68:71]\n"
    "buffer_load_dwordx4 v[224:227], v48, s[48:51], s85 offen\n"
    "v_perm_b32 v38, v148, v144, v63\n"
    "v_mfma_f32_16x16x32_bf16 a[128:131], v[32:35], a[8:11], a[128:131]\n"
    "v_perm_b32 v39, v156, v152, v63\n"
    "v_xor_b32 v1, s86, v7\n"
    "v_mfma_f32_16x16x32_bf16 a[132:135], v[32:35], a[12:15], a[132:135]\n"
    "ds_read_b128 a[16:19], v1 offset:0\n"
    "v_perm_b32 v40, v133, v129, v62\n"
    "v_mfma_f32_16x16x32_bf16 v[72:75], v[36:39], a[0:3], v[72:75]\n"
    "buffer_load_dwordx4 v[228:231], v49, s[48:51], s85 offen\n"
    "v_perm_b32 v41, v141, v137, v62\n"
    "v_mfma_f32_16x16x32_bf16 v[76:79], v[36:39], a[4:7], v[76:79]\n"
    "v_perm_b32 v42, v149, v145, v62\n"
    "v_xor_b32 v2, s86, v6\n"
    "v_mfma_f32_16x16x32_bf16 a[136:139], v[36:39], a[8:11], a[136:139]\n"
    "ds_read_b128 a[20:23], v1 offset:16384\n"
    "v_perm_b32 v43, v157, v153, v62\n"
    "v_mfma_f32_16x16x32_bf16 a[140:143], v[36:39], a[12:15], a[140:143]\n"
    "buffer_load_dwordx4 v[232:235], v50, s[48:51], s85 offen\n"
    "v_perm_b32 v44, v133, v129, v63\n"
    "v_mfma_f32_16x16x32_bf16 v[80:83], v[40:43], a[0:3], v[80:83]\n"
    "v_perm_b32 v45, v141, v137, v63\n"
    "v_mfma_f32_16x16x32_bf16 v[84:87], v[40:43], a[4:7], v[84:87]\n"
    "ds_read_b128 a[24:27], v2 offset:0\n"
    "v_perm_b32 v46, v149, v145, v63\n"
    "v_mfma_f32_16x16x32_bf16 a[144:147], v[40:43], a[8:11], a[144:147]\n"
    "buffer_load_dwordx4 v[236:239], v51, s[48:51], s85 offen\n"
    "v_perm_b32 v47, v157, v153, v63\n"
    "v_mfma_f32_16x16x32_bf16 a[148:151], v[40:43], a[12:15], a[148:151]\n"
    "v_perm_b32 v32, v134, v130, v62\n"
    "v_mfma_f32_16x16x32_bf16 v[88:91], v[44:47], a[0:3], v[88:91]\n"
    "ds_read_b128 a[28:31], v2 offset:16384\n"
    "v_perm_b32 v33, v142, v138, v62\n"
    "v_mfma_f32_16x16x32_bf16 v[92:95], v[44:47], a[4:7], v[92:95]\n"
    "buffer_load_dwordx4 v[240:243], v52, s[48:51], s85 offen\n"
    "v_perm_b32 v34, v150, v146, v62\n"
    "v_mfma_f32_16x16x32_bf16 a[152:155], v[44:47], a[8:11], a[152:155]\n"
    "s_add_u32 s86, s86, 64\n"
    "v_perm_b32 v35, v158, v154, v62\n"
    "v_mfma_f32_16x16x32_bf16 a[156:159], v[44:47], a[12:15], a[156:159]\n"
    "s_and_b32 s86, s86, 0x3c0\n"
    "v_perm_b32 v36, v134, v130, v63\n"
    "v_mfma_f32_16x16x32_bf16 v[96:99], v[32:35], a[0:3], v[96:99]\n"
    "buffer_load_dwordx4 v[244:247], v53, s[48:51], s85 offen\n"
    "v_perm_b32 v37, v142, v138, v63\n"
    "v_mfma_f32_16x16x32_bf16 v[100:103], v[32:35], a[4:7], v[100:103]\n"
    "v_perm_b32 v38, v150, v146, v63\n"
    "v_mfma_f32_16x16x32_bf16 a[160:163], v[32:35], a[8:11], a[160:163]\n"
    "v_perm_b32 v39, v158, v154, v63\n"
    "v_mfma_f32_16x16x32_bf16 a[164:167], v[32:35], a[12:15], a[164:167]\n"
    "buffer_load_dwordx4 v[248:251], v54, s[48:51], s85 offen\n"
    "v_perm_b32 v40, v135, v131, v62\n"
    "v_mfma_f32_16x16x32_bf16 v[104:107], v[36:39], a[0:3], v[104:107]\n"
    "v_perm_b32 v41, v143, v139, v62\n"
    "v_mfma_f32_16x16x32_bf16 v[108:111], v[36:39], a[4:7], v[108:111]\n"
    "v_perm_b32 v42, v151, v147, v62\n"
    "v_mfma_f32_16x16x32_bf16 a[168:171], v[36:39], a[8:11], a[168:171]\n"
    "buffer_load_dwordx4 v[252:255], v55, s[48:51], s85 offen\n"
    "v_perm_b32 v43, v159, v155, v62\n"
    "v_mfma_f32_16x16x32_bf16 a[172:175], v[36:39], a[12:15], a[172:175]\n"
    "s_add_u32 s85, s85, s90\n"
    "v_perm_b32 v44, v135, v131, v63\n"
    "v_mfma_f32_16x16x32_bf16 v[112:115], v[40:43], a[0:3], v[112:115]\n"
    "s_and_b32 s85, s85, s91\n"
    "v_perm_b32 v45, v143, v139, v63\n"
    "v_mfma_f32_16x16x32_bf16 v[116:119], v[40:43], a[4:7], v[116:119]\n"
    "s_bitcmp1_b32 s70, s83\n"
    "v_perm_b32 v46, v151, v147, v63\n"
    "v_mfma_f32_16x16x32_bf16 a[176:179], v[40:43], a[8:11], a[176:179]\n"
    "s_cbranch_scc1 Lq0h_%=\n"
    "s_waitcnt vmcnt(16) lgkmcnt(0)\n"
    "Lq0w_%=:\n"
    "v_perm_b32 v47, v159, v155, v63\n"
    "v_mfma_f32_16x16x32_bf16 a[180:183], v[40:43], a[12:15], a[180:183]\n"
    "v_perm_b32 v32, v164, v160, v62\n"
    "v_mfma_f32_16x16x32_bf16 v[120:123], v[44:47], a[0:3], v[120:123]\n"
    "v_perm_b32 v33, v172, v168, v62\n"
    "v_mfma_f32_16x16x32_bf16 v[124:127], v[44:47], a[4:7], v[124:127]\n"
    "s_add_u32 s83, s83, 1\n"
    "v_perm_b32 v34, v180, v176, v62\n"
    "v_mfma_f32_16x16x32_bf16 a[184:187], v[44:47], a[8:11], a[184:187]\n"
    "v_perm_b32 v35, v188, v184, v62\n"
    "v_mfma_f32_16x16x32_bf16 a[188:191], v[44:47], a[12:15], a[188:191]\n"
    "v_perm_b32 v36, v164, v160, v63\n"
    "v_mfma_f32_16x16x32_bf16 v[64:67], v[32:35], a[16:19], v[64:67]\n"
    "v_perm_b32 v37, v172, v168, v63\n"
    "v_mfma_f32_16x16x32_bf16 v[68:71], v[32:35], a[20:23], v[68:71]\n"
    "buffer_load_dwordx4 v[128:131], v48, s[48:51], s85 offen\n"
    "v_perm_b32 v38, v180, v176, v63\n"
    "v_mfma_f32_16x16x32_bf16 a[128:131], v[32:35], a[24:27], a[128:131]\n"
    "v_perm_b32 v39, v188, v184, v63\n"
    "v_xor_b32 v1, s86, v7\n"
    "v_mfma_f32_16x16x32_bf16 a[132:135], v[32:35], a[28:31], a[132:135]\n"
    "ds_read_b128 a[0:3], v1 offset:0\n"
    "v_perm_b32 v40, v165, v161, v62\n"
    "v_mfma_f32_16x16x32_bf16 v[72:75], v[36:39], a[16:19], v[72:75]\n"
    "buffer_load_dwordx4 v[132:135], v49, s[48:51], s85 offen\n"
    "v_perm_b32 v41, v173, v169, v62\n"
    "v_mfma_f32_16x16x32_bf16 v[76:79], v[36:39], a[20:23], v[76:79]\n"
    "v_perm_b32 v42, v181, v177, v62\n"
    "v_xor_b32 v2, s86, v6\n"
    "v_mfma_f32_16x16x32_bf16 a[136:139], v[36:39], a[24:27], a[136:139]\n"
    "ds_read_b128 a[4:7], v1 offset:16384\n"
    "v_perm_b32 v43, v189, v185, v62\n"
    "v_mfma_f32_16x16x32_bf16 a[140:143], v[36:39], a[28:31], a[140:143]\n"
    "buffer_load_dwordx4 v[136:139], v50, s[48:51], s85 offen\n"
    "v_perm_b32 v44, v165, v161, v63\n"
    "v_mfma_f32_16x16x32_bf16 v[80:83], v[40:43], a[16:19], v[80:83]\n"
    "v_perm_b32 v45, v173, v169, v63\n"
    "v_mfma_f32_16x16x32_bf16 v[84:87], v[40:43], a[20:23], v[84:87]\n"
    "ds_read_b128 a[8:11], v2 offset:0\n"
    "v_perm_b32 v46, v181, v177, v63\n"
    "v_mfma_f32_16x16x32_bf16 a[144:147], v[40:43], a[24:27], a[144:147]\n"
    "buffer_load_dwordx4 v[140:143], v51, s[48:51], s85 offen\n"
    "v_perm_b32 v47, v189, v185, v63\n"
    "v_mfma_f32_16x16x32_bf16 a[148:151], v[40:43], a[28:31], a[148:151]\n"
    "v_perm_b32 v32, v166, v162, v62\n"
    "v_mfma_f32_16x16x32_bf16 v[88:91], v[44:47], a[16:19], v[88:91]\n"
    "ds_read_b128 a[12:15], v2 offset:16384\n"
    "v_perm_b32 v33, v174, v170, v62\n"
    "v_mfma_f32_16x16x32_bf16 v[92:95], v[44:47], a[20:23], v[92:95]\n"
    "buffer_load_dwordx4 v[144:147], v52, s[48:51], s85 offen\n"
    "v_perm_b32 v34, v182, v178, v62\n"
    "v_mfma_f32_16x16x32_bf16 a[152:155], v[44:47], a[24:27], a[152:155]\n"
    "s_add_u32 s86, s86, 64\n"
    "v_perm_b32 v35, v190, v186, v62\n"
    "v_mfma_f32_16x16x32_bf16 a[156:159], v[44:47], a[28:31], a[156:159]\n"
    "s_and_b32 s86, s86, 0x3c0\n"
    "v_perm_b32 v36, v166, v162, v63\n"
    "v_mfma_f32_16x16x32_bf16 v[96:99], v[32:35], a[16:19], v[96:99]\n"
    "buffer_load_dwordx4 v[148:151], v53, s[48:51], s85 offen\n"
    "v_perm_b32 v37, v174, v170, v63\n"
    "v_mfma_f32_16x16x32_bf16 v[100:103], v[32:35], a[20:23], v[100:103]\n"
    "v_perm_b32 v38, v182, v178, v63\n"
    "v_mfma_f32_16x16x32_bf16 a[160:163], v[32:35], a[24:27], a[160:163]\n"
    "v_perm_b32 v39, v190, v186, v63\n"
    "v_mfma_f32_16x16x32_bf16 a[164:167], v[32:35], a[28:31], a[164:167]\n"
    "buffer_load_dwordx4 v[152:155], v54, s[48:51], s85 offen\n"
    "v_perm_b32 v40, v167, v163, v62\n"
    "v_mfma_f32_16x16x32_bf16 v[104:107], v[36:39], a[16:19], v[104:107]\n"
    "v_perm_b32 v41, v175, v171, v62\n"
    "v_mfma_f32_16x16x32_bf16 v[108:111], v[36:39], a[20:23], v[108:111]\n"
    "v_perm_b32 v42, v183, v179, v62\n"
    "v_mfma_f32_16x16x32_bf16 a[168:171], v[36:39], a[24:27], a[168:171]\n"
    "buffer_load_dwordx4 v[156:159], v55, s[48:51], s85 offen\n"
    "v_perm_b32 v43, v191, v187, v62\n"
    "v_mfma_f32_16x16x32_bf16 a[172:175], v[36:39], a[28:31], a[172:175]\n"
    "s_add_u32 s85, s85, s90\n"
    "v_perm_b32 v44, v167, v163, v63\n"
    "v_mfma_f32_16x16x32_bf16 v[112:115], v[40:43], a[16:19], v[112:115]\n"
    "s_and_b32 s85, s85, s91\n"
    "v_perm_b32 v45, v175, v171, v63\n"
    "v_mfma_f32_16x16x32_bf16 v[116:119], v[40:43], a[20:23], v[116:119]\n"
    "s_bitcmp1_b32 s70, s83\n"
    "v_perm_b32 v46, v183, v179, v63\n"
    "v_mfma_f32_16x16x32_bf16 a[176:179], v[40:43], a[24:27], a[176:179]\n"
    "s_cbranch_scc1 Lq1h_%=\n"
    "s_waitcnt vmcnt(16) lgkmcnt(0)\n"
    "Lq1w_%=:\n"
    "v_perm_b32 v47, v191, v187, v63\n"
    "v_mfma_f32_16x16x32_bf16 a[180:183], v[40:43], a[28:31], a[180:183]\n"
    "v_perm_b32 v32, v196, v192, v62\n"
    "v_mfma_f32_16x16x32_bf16 v[120:123], v[44:47], a[16:19], v[120:123]\n"
    "v_perm_b32 v33, v204, v200, v62\n"
    "v_mfma_f32_16x16x32_bf16 v[124:127], v[44:47], a[20:23], v[124:127]\n"
    "s_add_u32 s83, s83, 1\n"
    "v_perm_b32 v34, v212, v208, v62\n"
    "v_mfma_f32_16x16x32_bf16 a[184:187], v[44:47], a[24:27], a[184:187]\n"
    "v_perm_b32 v35, v220, v216, v62\n"
    "v_mfma_f32_16x16x32_bf16 a[188:191], v[44:47], a[28:31], a[188:191]\n"
    "v_perm_b32 v36, v196, v192, v63\n"
    "v_mfma_f32_16x16x32_bf16 v[64:67], v[32:35], a[0:3], v[64:67]\n"
    "v_perm_b32 v37, v204, v200, v63\n"
    "v_mfma_f32_16x16x32_bf16 v[68:71], v[32:35], a[4:7], v[68:71]\n"
    "buffer_load_dwordx4 v[160:163], v48, s[48:51], s85 offen\n"
    "v_perm_b32 v38, v212, v208, v63\n"
    "v_mfma_f32_16x16x32_bf16 a[128:131], v[32:35], a[8:11], a[128:131]\n"
    "v_perm_b32 v39, v220, v216, v63\n"
    "v_xor_b32 v1, s86, v7\n"
    "v_mfma_f32_16x16x32_bf16 a[132:135], v[32:35], a[12:15], a[132:135]\n"
    "ds_read_b128 a[16:19], v1 offset:0\n"
    "v_perm_b32 v40, v197, v193, v62\n"
    "v_mfma_f32_16x16x32_bf16 v[72:75], v[36:39], a[0:3], v[72:75]\n"
    "buffer_load_dwordx4 v[164:167], v49, s[48:51], s85 offen\n"
    "v_perm_b32 v41, v205, v201, v62\n"
    "v_mfma_f32_16x16x32_bf16 v[76:79], v[36:39], a[4:7], v[76:79]\n"
    "v_perm_b32 v42, v213, v209, v62\n"
    "v_xor_b32 v2, s86, v6\n"
    "v_mfma_f32_16x16x32_bf16 a[136:139], v[36:39], a[8:11], a[136:139]\n"
    "ds_read_b128 a[20:23], v1 offset:16384\n"
    "v_perm_b32 v43, v221, v217, v62\n"
    "v_mfma_f32_16x16x32_bf16 a[140:143], v[36:39], a[12:15], a[140:143]\n"
    "buffer_load_dwordx4 v[168:171], v50, s[48:51], s85 offen\n"
    "v_perm_b32 v44, v197, v193, v63\n"
    "v_mfma_f32_16x16x32_bf16 v[80:83], v[40:43], a[0:3], v[80:83]\n"
    "v_perm_b32 v45, v205, v201, v63\n"
    "v_mfma_f32_16x16x32_bf16 v[84:87], v[40:43], a[4:7], v[84:87]\n"
    "ds_read_b128 a[24:27], v2 offset:0\n"
    "v_perm_b32 v46, v213, v209, v63\n"
    "v_mfma_f32_16x16x32_bf16 a[144:147], v[40:43], a[8:11], a[144:147]\n"
    "buffer_load_dwordx4 v[172:175], v51, s[48:51], s85 offen\n"
    "v_perm_b32 v47, v221, v217, v63\n"
    "v_mfma_f32_16x16x32_bf16 a[148:151], v[40:43], a[12:15], a[148:151]\n"
    "v_perm_b32 v32, v198, v194, v62\n"
    "v_mfma_f32_16x16x32_bf16 v[88:91], v[44:47], a[0:3], v[88:91]\n"
    "ds_read_b128 a[28:31], v2 offset:16384\n"
    "v_perm_b32 v33, v206, v202, v62\n"
    "v_mfma_f32_16x16x32_bf16 v[92:95], v[44:47], a[4:7], v[92:95]\n"
    "buffer_load_dwordx4 v[176:179], v52, s[48:51], s85 offen\n"
    "v_perm_b32 v34, v214, v210, v62\n"
    "v_mfma_f32_16x16x32_bf16 a[152:155], v[44:47], a[8:11], a[152:155]\n"
    "s_add_u32 s86, s86, 64\n"
    "v_perm_b32 v35, v222, v218, v62\n"
    "v_mfma_f32_16x16x32_bf16 a[156:159], v[44:47], a[12:15], a[156:159]\n"
    "s_and_b32 s86, s86, 0x3c0\n"
    "v_perm_b32 v36, v198, v194, v63\n"
    "v_mfma_f32_16x16x32_bf16 v[96:99], v[32:35], a[0:3], v[96:99]\n"
    "buffer_load_dwordx4 v[180:183], v53, s[48:51], s85 offen\n"
    "v_perm_b32 v37, v206, v202, v63\n"
    "v_mfma_f32_16x16x32_bf16 v[100:103], v[32:35], a[4:7], v[100:103]\n"
    "v_perm_b32 v38, v214, v210, v63\n"
    "v_mfma_f32_16x16x32_bf16 a[160:163], v[32:35], a[8:11], a[160:163]\n"
    "v_perm_b32 v39, v222, v218, v63\n"
    "v_mfma_f32_16x16x32_bf16 a[164:167], v[32:35], a[12:15], a[164:167]\n"
    "buffer_load_dwordx4 v[184:187], v54, s[48:51], s85 offen\n"
    "v_perm_b32 v40, v199, v195, v62\n"
    "v_mfma_f32_16x16x32_bf16 v[104:107], v[36:39], a[0:3], v[104:107]\n"
    "v_perm_b32 v41, v207, v203, v62\n"
    "v_mfma_f32_16x16x32_bf16 v[108:111], v[36:39], a[4:7], v[108:111]\n"
    "v_perm_b32 v42, v215, v211, v62\n"
    "v_mfma_f32_16x16x32_bf16 a[168:171], v[36:39], a[8:11], a[168:171]\n"
    "buffer_load_dwordx4 v[188:191], v55, s[48:51], s85 offen\n"
    "v_perm_b32 v43, v223, v219, v62\n"
    "v_mfma_f32_16x16x32_bf16 a[172:175], v[36:39], a[12:15], a[172:175]\n"
    "s_add_u32 s85, s85, s90\n"
    "v_perm_b32 v44, v199, v195, v63\n"
    "v_mfma_f32_16x16x32_bf16 v[112:115], v[40:43], a[0:3], v[112:115]\n"
    "s_and_b32 s85, s85, s91\n"
    "v_perm_b32 v45, v207, v203, v63\n"
    "v_mfma_f32_16x16x32_bf16 v[116:119], v[40:43], a[4:7], v[116:119]\n"
    "s_bitcmp1_b32 s70, s83\n"
    "v_perm_b32 v46, v215, v211, v63\n"
    "v_mfma_f32_16x16x32_bf16 a[176:179], v[40:43], a[8:11], a[176:179]\n"
    "s_cbranch_scc1 Lq2h_%=\n"
    "s_waitcnt vmcnt(16) lgkmcnt(0)\n"
    "Lq2w_%=:\n"
    "v_perm_b32 v47, v223, v219, v63\n"
    "v_mfma_f32_16x16x32_bf16 a[180:183], v[40:43], a[12:15], a[180:183]\n"
    "v_perm_b32 v32, v228, v224, v62\n"
    "v_mfma_f32_16x16x32_bf16 v[120:123], v[44:47], a[0:3], v[120:123]\n"
    "v_perm_b32 v33, v236, v232, v62\n"
    "v_mfma_f32_16x16x32_bf16 v[124:127], v[44:47], a[4:7], v[124:127]\n"
    "s_add_u32 s83, s83, 1\n"
    "v_perm_b32 v34, v244, v240, v62\n"
    "v_mfma_f32_16x16x32_bf16 a[184:187], v[44:47], a[8:11], a[184:187]\n"
    "v_perm_b32 v35, v252, v248, v62\n"
    "v_mfma_f32_16x16x32_bf16 a[188:191], v[44:47], a[12:15], a[188:191]\n"
    "v_perm_b32 v36, v228, v224, v63\n"
    "v_mfma_f32_16x16x32_bf16 v[64:67], v[32:35], a[16:19], v[64:67]\n"
    "v_perm_b32 v37, v236, v232, v63\n"
    "v_mfma_f32_16x16x32_bf16 v[68:71], v[32:35], a[20:23], v[68:71]\n"
    "buffer_load_dwordx4 v[192:195], v48, s[48:51], s85 offen\n"
    "v_perm_b32 v38, v244, v240, v63\n"
    "v_mfma_f32_16x16x32_bf16 a[128:131], v[32:35], a[24:27], a[128:131]\n"
    "v_perm_b32 v39, v252, v248, v63\n"
    "v_xor_b32 v1, s86, v7\n"
    "v_mfma_f32_16x16x32_bf16 a[132:135], v[32:35], a[28:31], a[132:135]\n"
    "ds_read_b128 a[0:3], v1 offset:0\n"
    "v_perm_b32 v40, v229, v225, v62\n"
    "v_mfma_f32_16x16x32_bf16 v[72:75], v[36:39], a[16:19], v[72:75]\n"
    "buffer_load_dwordx4 v[196:199], v49, s[48:51], s85 offen\n"
    "v_perm_b32 v41, v237, v233, v62\n"
    "v_mfma_f32_16x16x32_bf16 v[76:79], v[36:39], a[20:23], v[76:79]\n"
    "v_perm_b32 v42, v245, v241, v62\n"
    "v_xor_b32 v2, s86, v6\n"
    "v_mfma_f32_16x16x32_bf16 a[136:139], v[36:39], a[24:27], a[136:139]\n"
    "ds_read_b128 a[4:7], v1 offset:16384\n"
    "v_perm_b32 v43, v253, v249, v62\n"
    "v_mfma_f32_16x16x32_bf16 a[140:143], v[36:39], a[28:31], a[140:143]\n"
    "buffer_load_dwordx4 v[200:203], v50, s[48:51], s85 offen\n"
    "v_perm_b32 v44, v229, v225, v63\n"
    "v_mfma_f32_16x16x32_bf16 v[80:83], v[40:43], a[16:19], v[80:83]\n"
    "v_perm_b32 v45, v237, v233, v63\n"
    "v_mfma_f32_16x16x32_bf16 v[84:87], v[40:43], a[20:23], v[84:87]\n"
    "ds_read_b128 a[8:11], v2 offset:0\n"
    "v_perm_b32 v46, v245, v241, v63\n"
    "v_mfma_f32_16x16x32_bf16 a[144:147], v[40:43], a[24:27], a[144:147]\n"
    "buffer_load_dwordx4 v[204:207], v51, s[48:51], s85 offen\n"
    "v_perm_b32 v47, v253, v249, v63\n"
    "v_mfma_f32_16x16x32_bf16 a[148:151], v[40:43], a[28:31], a[148:151]\n"
    "v_perm_b32 v32, v230, v226, v62\n"
    "v_mfma_f32_16x16x32_bf16 v[88:91], v[44:47], a[16:19], v[88:91]\n"
    "ds_read_b128 a[12:15], v2 offset:16384\n"
    "v_perm_b32 v33, v238, v234, v62\n"
    "v_mfma_f32_16x16x32_bf16 v[92:95], v[44:47], a[20:23], v[92:95]\n"
    "buffer_load_dwordx4 v[208:211], v52, s[48:51], s85 offen\n"
    "v_perm_b32 v34, v246, v242, v62\n"
    "v_mfma_f32_16x16x32_bf16 a[152:155], v[44:47], a[24:27], a[152:155]\n"
    "s_add_u32 s86, s86, 64\n"
    "v_perm_b32 v35, v254, v250, v62\n"
    "v_mfma_f32_16x16x32_bf16 a[156:159], v[44:47], a[28:31], a[156:159]\n"
    "s_and_b32 s86, s86, 0x3c0\n"
    "v_perm_b32 v36, v230, v226, v63\n"
    "v_mfma_f32_16x16x32_bf16 v[96:99], v[32:35], a[16:19], v[96:99]\n"
    "buffer_load_dwordx4 v[212:215], v53, s[48:51], s85 offen\n"
    "v_perm_b32 v37, v238, v234, v63\n"
    "v_mfma_f32_16x16x32_bf16 v[100:103], v[32:35], a[20:23], v[100:103]\n"
    "v_perm_b32 v38, v246, v242, v63\n"
    "v_mfma_f32_16x16x32_bf16 a[160:163], v[32:35], a[24:27], a[160:163]\n"
    "v_perm_b32 v39, v254, v250, v63\n"
    "v_mfma_f32_16x16x32_bf16 a[164:167], v[32:35], a[28:31], a[164:167]\n"
    "buffer_load_dwordx4 v[216:219], v54, s[48:51], s85 offen\n"
    "v_perm_b32 v40, v231, v227, v62\n"
    "v_mfma_f32_16x16x32_bf16 v[104:107], v[36:39], a[16:19], v[104:107]\n"
    "v_perm_b32 v41, v239, v235, v62\n"
    "v_mfma_f32_16x16x32_bf16 v[108:111], v[36:39], a[20:23], v[108:111]\n"
    "v_perm_b32 v42, v247, v243, v62\n"
    "v_mfma_f32_16x16x32_bf16 a[168:171], v[36:39], a[24:27], a[168:171]\n"
    "buffer_load_dwordx4 v[220:223], v55, s[48:51], s85 offen\n"
    "v_perm_b32 v43, v255, v251, v62\n"
    "v_mfma_f32_16x16x32_bf16 a[172:175], v[36:39], a[28:31], a[172:175]\n"
    "s_add_u32 s85, s85, s90\n"
    "v_perm_b32 v44, v231, v227, v63\n"
    "v_mfma_f32_16x16x32_bf16 v[112:115], v[40:43], a[16:19], v[112:115]\n"
    "s_and_b32 s85, s85, s91\n"
    "v_perm_b32 v45, v239, v235, v63\n"
    "v_mfma_f32_16x16x32_bf16 v[116:119], v[40:43], a[20:23], v[116:119]\n"
    "s_bitcmp1_b32 s70, s83\n"
    "v_perm_b32 v46, v247, v243, v63\n"
    "v_mfma_f32_16x16x32_bf16 a[176:179], v[40:43], a[24:27], a[176:179]\n"
    "s_cbranch_scc1 Lq3h_%=\n"
    "s_waitcnt vmcnt(16) lgkmcnt(0)\n"
    "Lq3w_%=:\n"
    "v_perm_b32 v47, v255, v251, v63\n"
    "v_mfma_f32_16x16x32_bf16 a[180:183], v[40:43], a[28:31], a[180:183]\n"
    "v_perm_b32 v32, v132, v128, v62\n"
    "v_mfma_f32_16x16x32_bf16 v[120:123], v[44:47], a[16:19], v[120:123]\n"
    "v_perm_b32 v33, v140, v136, v62\n"
    "v_mfma_f32_16x16x32_bf16 v[124:127], v[44:47], a[20:23], v[124:127]\n"
    "s_add_u32 s83, s83, 1\n"
    "v_perm_b32 v34, v148, v144, v62\n"
    "v_mfma_f32_16x16x32_bf16 a[184:187], v[44:47], a[24:27], a[184:187]\n"
    "v_perm_b32 v35, v156, v152, v62\n"
    "v_mfma_f32_16x16x32_bf16 a[188:191], v[44:47], a[28:31], a[188:191]\n"
    "v_perm_b32 v36, v132, v128, v63\n"
    "s_cmp_lt_u32 s83, 12\n"
    "s_cbranch_scc1 Lq_%=\n"
    "v_mfma_f32_16x16x32_bf16 v[64:67], v[32:35], a[0:3], v[64:67]\n"
    "v_perm_b32 v37, v140, v136, v63\n"
    "v_mfma_f32_16x16x32_bf16 v[68:71], v[32:35], a[4:7], v[68:71]\n"
    "buffer_load_dwordx4 v[224:227], v48, s[48:51], s85 offen\n"
    "v_perm_b32 v38, v148, v144, v63\n"
    "v_mfma_f32_16x16x32_bf16 a[128:131], v[32:35], a[8:11], a[128:131]\n"
    "v_perm_b32 v39, v156, v152, v63\n"
    "v_xor_b32 v1, s86, v7\n"
    "v_mfma_f32_16x16x32_bf16 a[132:135], v[32:35], a[12:15], a[132:135]\n"
    "ds_read_b128 a[16:19], v1 offset:0\n"
    "v_perm_b32 v40, v133, v129, v62\n"
    "v_mfma_f32_16x16x32_bf16 v[72:75], v[36:39], a[0:3], v[72:75]\n"
    "buffer_load_dwordx4 v[228:231], v49, s[48:51], s85 offen\n"
    "v_perm_b32 v41, v141, v137, v62\n"
    "v_mfma_f32_16x16x32_bf16 v[76:79], v[36:39], a[4:7], v[76:79]\n"
    "v_perm_b32 v42, v149, v145, v62\n"
    "v_xor_b32 v2, s86, v6\n"
    "v_mfma_f32_16x16x32_bf16 a[136:139], v[36:39], a[8:11], a[136:139]\n"
    "ds_read_b128 a[20:23], v1 offset:16384\n"
    "v_perm_b32 v43, v157, v153, v62\n"
    "v_mfma_f32_16x16x32_bf16 a[140:143], v[36:39], a[12:15], a[140:143]\n"
    "buffer_load_dwordx4 v[232:235], v50, s[48:51], s85 offen\n"
    "v_perm_b32 v44, v133, v129, v63\n"
    "v_mfma_f32_16x16x32_bf16 v[80:83], v[40:43], a[0:3], v[80:83]\n"
    "v_perm_b32 v45, v141, v137, v63\n"
    "v_mfma_f32_16x16x32_bf16 v[84:87], v[40:43], a[4:7], v[84:87]\n"
    "ds_read_b128 a[24:27], v2 offset:0\n"
    "v_perm_b32 v46, v149, v145, v63\n"
    "v_mfma_f32_16x16x32_bf16 a[144:147], v[40:43], a[8:11], a[144:147]\n"
    "buffer_load_dwordx4 v[236:239], v51, s[48:51], s85 offen\n"
    "v_perm_b32 v47, v157, v153, v63\n"
    "v_mfma_f32_16x16x32_bf16 a[148:151], v[40:43], a[12:15], a[148:151]\n"
    "v_perm_b32 v32, v134, v130, v62\n"
    "v_mfma_f32_16x16x32_bf16 v[88:91], v[44:47], a[0:3], v[88:91]\n"
    "ds_read_b128 a[28:31], v2 offset:16384\n"
    "v_perm_b32 v33, v142, v138, v62\n"
    "v_mfma_f32_16x16x32_bf16 v[92:95], v[44:47], a[4:7], v[92:95]\n"
    "buffer_load_dwordx4 v[240:243], v52, s[48:51], s85 offen\n"
    "v_perm_b32 v34, v150, v146, v62\n"
    "v_mfma_f32_16x16x32_bf16 a[152:155], v[44:47], a[8:11], a[152:155]\n"
    "s_add_u32 s86, s86, 64\n"
    "v_perm_b32 v35, v158, v154, v62\n"
    "v_mfma_f32_16x16x32_bf16 a[156:159], v[44:47], a[12:15], a[156:159]\n"
    "s_and_b32 s86, s86, 0x3c0\n"
    "v_perm_b32 v36, v134, v130, v63\n"
    "v_mfma_f32_16x16x32_bf16 v[96:99], v[32:35], a[0:3], v[96:99]\n"
    "buffer_load_dwordx4 v[244:247], v53, s[48:51], s85 offen\n"
    "v_perm_b32 v37, v142, v138, v63\n"
    "v_mfma_f32_16x16x32_bf16 v[100:103], v[32:35], a[4:7], v[100:103]\n"
    "v_perm_b32 v38, v150, v146, v63\n"
    "v_mfma_f32_16x16x32_bf16 a[160:163], v[32:35], a[8:11], a[160:163]\n"
    "v_perm_b32 v39, v158, v154, v63\n"
    "v_mfma_f32_16x16x32_bf16 a[164:167], v[32:35], a[12:15], a[164:167]\n"
    "buffer_load_dwordx4 v[248:251], v54, s[48:51], s85 offen\n"
    "v_perm_b32 v40, v135, v131, v62\n"
    "v_mfma_f32_16x16x32_bf16 v[104:107], v[36:39], a[0:3], v[104:107]\n"
    "v_perm_b32 v41, v143, v139, v62\n"
    "v_mfma_f32_16x16x32_bf16 v[108:111], v[36:39], a[4:7], v[108:111]\n"
    "v_perm_b32 v42, v151, v147, v62\n"
    "v_mfma_f32_16x16x32_bf16 a[168:171], v[36:39], a[8:11], a[168:171]\n"
    "buffer_load_dwordx4 v[252:255], v55, s[48:51], s85 offen\n"
    "v_perm_b32 v43, v159, v155, v62\n"
    "v_mfma_f32_16x16x32_bf16 a[172:175], v[36:39], a[12:15], a[172:175]\n"
    "s_add_u32 s85, s85, s90\n"
    "v_perm_b32 v44, v135, v131, v63\n"
    "v_mfma_f32_16x16x32_bf16 v[112:115], v[40:43], a[0:3], v[112:115]\n"
    "s_and_b32 s85, s85, s91\n"
    "v_perm_b32 v45, v143, v139, v63\n"
    "v_mfma_f32_16x16x32_bf16 v[116:119], v[40:43], a[4:7], v[116:119]\n"
    "s_bitcmp1_b32 s70, s83\n"
    "v_perm_b32 v46, v151, v147, v63\n"
    "v_mfma_f32_16x16x32_bf16 a[176:179], v[40:43], a[8:11], a[176:179]\n"
    "s_cbranch_scc1 Lqp0h_%=\n"
    "s_waitcnt vmcnt(16) lgkmcnt(0)\n"
    "Lqp0w_%=:\n"
    "v_perm_b32 v47, v159, v155, v63\n"
    "v_mfma_f32_16x16x32_bf16 a[180:183], v[40:43], a[12:15], a[180:183]\n"
    "v_perm_b32 v32, v164, v160, v62\n"
    "v_mfma_f32_16x16x32_bf16 v[120:123], v[44:47], a[0:3], v[120:123]\n"
    "v_perm_b32 v33, v172, v168, v62\n"
    "v_mfma_f32_16x16x32_bf16 v[124:127], v[44:47], a[4:7], v[124:127]\n"
    "s_add_u32 s83, s83, 1\n"
    "v_perm_b32 v34, v180, v176, v62\n"
    "v_mfma_f32_16x16x32_bf16 a[184:187], v[44:47], a[8:11], a[184:187]\n"
    "v_perm_b32 v35, v188, v184, v62\n"
    "v_mfma_f32_16x16x32_bf16 a[188:191], v[44:47], a[12:15], a[188:191]\n"
    "v_perm_b32 v36, v164, v160, v63\n"
    "v_mfma_f32_16x16x32_bf16 v[64:67], v[32:35], a[16:19], v[64:67]\n"
    "v_perm_b32 v37, v172, v168, v63\n"
    "v_mfma_f32_16x16x32_bf16 v[68:71], v[32:35], a[20:23], v[68:71]\n"
    "v_perm_b32 v38, v180, v176, v63\n"
    "v_mfma_f32_16x16x32_bf16 a[128:131], v[32:35], a[24:27], a[128:131]\n"
    "v_perm_b32 v39, v188, v184, v63\n"
    "v_xor_b32 v1, s86, v7\n"
    "v_mfma_f32_16x16x32_bf16 a[132:135], v[32:35], a[28:31], a[132:135]\n"
    "ds_read_b128 a[0:3], v1 offset:0\n"
    "v_perm_b32 v40, v165, v161, v62\n"
    "v_mfma_f32_16x16x32_bf16 v[72:75], v[36:39], a[16:19], v[72:75]\n"
    "v_perm_b32 v41, v173, v169, v62\n"
    "v_mfma_f32_16x16x32_bf16 v[76:79], v[36:39], a[20:23], v[76:79]\n"
    "v_perm_b32 v42, v181, v177, v62\n"
    "v_xor_b32 v2, s86, v6\n"
    "v_mfma_f32_16x16x32_bf16 a[136:139], v[36:39], a[24:27], a[136:139]\n"
    "ds_read_b128 a[4:7], v1 offset:16384\n"
    "v_perm_b32 v43, v189, v185, v62\n"
    "v_mfma_f32_16x16x32_bf16 a[140:143], v[36:39], a[28:31], a[140:143]\n"
    "v_perm_b32 v44, v165, v161, v63\n"
    "v_mfma_f32_16x16x32_bf16 v[80:83], v[40:43], a[16:19], v[80:83]\n"
    "v_perm_b32 v45, v173, v169, v63\n"
    "v_mfma_f32_16x16x32_bf16 v[84:87], v[40:43], a[20:23], v[84:87]\n"
    "ds_read_b128 a[8:11], v2 offset:0\n"
    "v_perm_b32 v46, v181, v177, v63\n"
    "v_mfma_f32_16x16x32_bf16 a[144:147], v[40:43], a[24:27], a[144:147]\n"
    "v_perm_b32 v47, v189, v185, v63\n"
    "v_mfma_f32_16x16x32_bf16 a[148:151], v[40:43], a[28:31], a[148:151]\n"
    "v_perm_b32 v32, v166, v162, v62\n"
    "v_mfma_f32_16x16x32_bf16 v[88:91], v[44:47], a[16:19], v[88:91]\n"
    "ds_read_b128 a[12:15], v2 offset:16384\n"
    "v_perm_b32 v33, v174, v170, v62\n"
    "v_mfma_f32_16x16x32_bf16 v[92:95], v[44:47], a[20:23], v[92:95]\n"
    "v_perm_b32 v34, v182, v178, v62\n"
    "v_mfma_f32_16x16x32_bf16 a[152:155], v[44:47], a[24:27], a[152:155]\n"
    "s_add_u32 s86, s86, 64\n"
    "v_perm_b32 v35, v190, v186, v62\n"
    "v_mfma_f32_16x16x32_bf16 a[156:159], v[44:47], a[28:31], a[156:159]\n"
    "s_and_b32 s86, s86, 0x3c0\n"
    "v_perm_b32 v36, v166, v162, v63\n"
    "v_mfma_f32_16x16x32_bf16 v[96:99], v[32:35], a[16:19], v[96:99]\n"
    "v_perm_b32 v37, v174, v170, v63\n"
    "v_mfma_f32_16x16x32_bf16 v[100:103], v[32:35], a[20:23], v[100:103]\n"
    "v_perm_b32 v38, v182, v178, v63\n"
    "v_mfma_f32_16x16x32_bf16 a[160:163], v[32:35], a[24:27], a[160:163]\n"
    "v_perm_b32 v39, v190, v186, v63\n"
    "v_mfma_f32_16x16x32_bf16 a[164:167], v[32:35], a[28:31], a[164:167]\n"
    "v_perm_b32 v40, v167, v163, v62\n"
    "v_mfma_f32_16x16x32_bf16 v[104:107], v[36:39], a[16:19], v[104:107]\n"
    "v_perm_b32 v41, v175, v171, v62\n"
    "v_mfma_f32_16x16x32_bf16 v[108:111], v[36:39], a[20:23], v[108:111]\n"
    "v_perm_b32 v42, v183, v179, v62\n"
    "v_mfma_f32_16x16x32_bf16 a[168:171], v[36:39], a[24:27], a[168:171]\n"
    "v_perm_b32 v43, v191, v187, v62\n"
    "v_mfma_f32_16x16x32_bf16 a[172:175], v[36:39], a[28:31], a[172:175]\n"
    "v_perm_b32 v44, v167, v163, v63\n"
    "v_mfma_f32_16x16x32_bf16 v[112:115], v[40:43], a[16:19], v[112:115]\n"
    "v_perm_b32 v45, v175, v171, v63\n"
    "v_mfma_f32_16x16x32_bf16 v[116:119], v[40:43], a[20:23], v[116:119]\n"
    "s_bitcmp1_b32 s70, s83\n"
    "v_perm_b32 v46, v183, v179, v63\n"
    "v_mfma_f32_16x16x32_bf16 a[176:179], v[40:43], a[24:27], a[176:179]\n"
    "s_cbranch_scc1 Lqp1h_%=\n"
    "s_waitcnt vmcnt(8) lgkmcnt(0)\n"
    "Lqp1w_%=:\n"
    "v_perm_b32 v47, v191, v187, v63\n"
    "v_mfma_f32_16x16x32_bf16 a[180:183], v[40:43], a[28:31], a[180:183]\n"
    "v_perm_b32 v32, v196, v192, v62\n"
    "v_mfma_f32_16x16x32_bf16 v[120:123], v[44:47], a[16:19], v[120:123]\n"
    "v_perm_b32 v33, v204, v200, v62\n"
    "v_mfma_f32_16x16x32_bf16 v[124:127], v[44:47], a[20:23], v[124:127]\n"
    "s_add_u32 s83, s83, 1\n"
    "v_perm_b32 v34, v212, v208, v62\n"
    "v_mfma_f32_16x16x32_bf16 a[184:187], v[44:47], a[24:27], a[184:187]\n"
    "v_perm_b32 v35, v220, v216, v62\n"
    "v_mfma_f32_16x16x32_bf16 a[188:191], v[44:47], a[28:31], a[188:191]\n"
    "v_perm_b32 v36, v196, v192, v63\n"
    "v_mfma_f32_16x16x32_bf16 v[64:67], v[32:35], a[0:3], v[64:67]\n"
    "v_perm_b32 v37, v204, v200, v63\n"
    "v_mfma_f32_16x16x32_bf16 v[68:71], v[32:35], a[4:7], v[68:71]\n"
    "v_perm_b32 v38, v212, v208, v63\n"
    "v_mfma_f32_16x16x32_bf16 a[128:131], v[32:35], a[8:11], a[128:131]\n"
    "v_perm_b32 v39, v220, v216, v63\n"
    "v_xor_b32 v1, s86, v7\n"
    "v_mfma_f32_16x16x32_bf16 a[132:135], v[32:35], a[12:15], a[132:135]\n"
    "ds_read_b128 a[16:19], v1 offset:0\n"
    "v_perm_b32 v40, v197, v193, v62\n"
    "v_mfma_f32_16x16x32_bf16 v[72:75], v[36:39], a[0:3], v[72:75]\n"
    "v_perm_b32 v41, v205, v201, v62\n"
    "v_mfma_f32_16x16x32_bf16 v[76:79], v[36:39], a[4:7], v[76:79]\n"
    "v_perm_b32 v42, v213, v209, v62\n"
    "v_xor_b32 v2, s86, v6\n"
    "v_mfma_f32_16x16x32_bf16 a[136:139], v[36:39], a[8:11], a[136:139]\n"
    "ds_read_b128 a[20:23], v1 offset:16384\n"
    "v_perm_b32 v43, v221, v217, v62\n"
    "v_mfma_f32_16x16x32_bf16 a[140:143], v[36:39], a[12:15], a[140:143]\n"
    "v_perm_b32 v44, v197, v193, v63\n"
    "v_mfma_f32_16x16x32_bf16 v[80:83], v[40:43], a[0:3], v[80:83]\n"
    "v_perm_b32 v45, v205, v201, v63\n"
    "v_mfma_f32_16x16x32_bf16 v[84:87], v[40:43], a[4:7], v[84:87]\n"
    "ds_read_b128 a[24:27], v2 offset:0\n"
    "v_perm_b32 v46, v213, v209, v63\n"
    "v_mfma_f32_16x16x32_bf16 a[144:147], v[40:43], a[8:11], a[144:147]\n"
    "v_perm_b32 v47, v221, v217, v63\n"
    "v_mfma_f32_16x16x32_bf16 a[148:151], v[40:43], a[12:15], a[148:151]\n"
    "v_perm_b32 v32, v198, v194, v62\n"
    "v_mfma_f32_16x16x32_bf16 v[88:91], v[44:47], a[0:3], v[88:91]\n"
    "ds_read_b128 a[28:31], v2 offset:16384\n"
    "v_perm_b32 v33, v206, v202, v62\n"
    "v_mfma_f32_16x16x32_bf16 v[92:95], v[44:47], a[4:7], v[92:95]\n"
    "v_perm_b32 v34, v214, v210, v62\n"
    "v_mfma_f32_16x16x32_bf16 a[152:155], v[44:47], a[8:11], a[152:155]\n"
    "s_add_u32 s86, s86, 64\n"
    "v_perm_b32 v35, v222, v218, v62\n"
    "v_mfma_f32_16x16x32_bf16 a[156:159], v[44:47], a[12:15], a[156:159]\n"
    "s_and_b32 s86, s86, 0x3c0\n"
    "v_perm_b32 v36, v198, v194, v63\n"
    "v_mfma_f32_16x16x32_bf16 v[96:99], v[32:35], a[0:3], v[96:99]\n"
    "v_perm_b32 v37, v206, v202, v63\n"
    "v_mfma_f32_16x16x32_bf16 v[100:103], v[32:35], a[4:7], v[100:103]\n"
    "v_perm_b32 v38, v214, v210, v63\n"
    "v_mfma_f32_16x16x32_bf16 a[160:163], v[32:35], a[8:11], a[160:163]\n"
    "v_perm_b32 v39, v222, v218, v63\n"
    "v_mfma_f32_16x16x32_bf16 a[164:167], v[32:35], a[12:15], a[164:167]\n"
    "v_perm_b32 v40, v199, v195, v62\n"
    "v_mfma_f32_16x16x32_bf16 v[104:107], v[36:39], a[0:3], v[104:107]\n"
    "v_perm_b32 v41, v207, v203, v62\n"
    "v_mfma_f32_16x16x32_bf16 v[108:111], v[36:39], a[4:7], v[108:111]\n"
    "v_perm_b32 v42, v215, v211, v62\n"
    "v_mfma_f32_16x16x32_bf16 a[168:171], v[36:39], a[8:11], a[168:171]\n"
    "v_perm_b32 v43, v223, v219, v62\n"
    "v_mfma_f32_16x16x32_bf16 a[172:175], v[36:39], a[12:15], a[172:175]\n"
    "v_perm_b32 v44, v199, v195, v63\n"
    "v_mfma_f32_16x16x32_bf16 v[112:115], v[40:43], a[0:3], v[112:115]\n"
    "v_perm_b32 v45, v207, v203, v63\n"
    "v_mfma_f32_16x16x32_bf16 v[116:119], v[40:43], a[4:7], v[116:119]\n"
    "s_bitcmp1_b32 s70, s83\n"
    "v_perm_b32 v46, v215, v211, v63\n"
    "v_mfma_f32_16x16x32_bf16 a[176:179], v[40:43], a[8:11], a[176:179]\n"
    "s_cbranch_scc1 Lqp2h_%=\n"
    "s_waitcnt vmcnt(0) lgkmcnt(0)\n"
    "Lqp2w_%=:\n"
    "v_perm_b32 v47, v223, v219, v63\n"
    "v_mfma_f32_16x16x32_bf16 a[180:183], v[40:43], a[12:15], a[180:183]\n"
    "v_perm_b32 v32, v228, v224, v62\n"
    "v_mfma_f32_16x16x32_bf16 v[120:123], v[44:47], a[0:3], v[120:123]\n"
    "v_perm_b32 v33, v236, v232, v62\n"
    "v_mfma_f32_16x16x32_bf16 v[124:127], v[44:47], a[4:7], v[124:127]\n"
    "s_add_u32 s83, s83, 1\n"
    "v_perm_b32 v34, v244, v240, v62\n"
    "v_mfma_f32_16x16x32_bf16 a[184:187], v[44:47], a[8:11], a[184:187]\n"
    "v_perm_b32 v35, v252, v248, v62\n"
    "v_mfma_f32_16x16x32_bf16 a[188:191], v[44:47], a[12:15], a[188:191]\n"
    "v_perm_b32 v36, v228, v224, v63\n"
    "v_mfma_f32_16x16x32_bf16 v[64:67], v[32:35], a[16:19], v[64:67]\n"
    "v_perm_b32 v37, v236, v232, v63\n"
    "v_mfma_f32_16x16x32_bf16 v[68:71], v[32:35], a[20:23], v[68:71]\n"
    "v_perm_b32 v38, v244, v240, v63\n"
    "v_mfma_f32_16x16x32_bf16 a[128:131], v[32:35], a[24:27], a[128:131]\n"
    "v_perm_b32 v39, v252, v248, v63\n"
    "v_mfma_f32_16x16x32_bf16 a[132:135], v[32:35], a[28:31], a[132:135]\n"
    "v_perm_b32 v40, v229, v225, v62\n"
    "v_mfma_f32_16x16x32_bf16 v[72:75], v[36:39], a[16:19], v[72:75]\n"
    "v_perm_b32 v41, v237, v233, v62\n"
    "v_mfma_f32_16x16x32_bf16 v[76:79], v[36:39], a[20:23], v[76:79]\n"
    "v_perm_b32 v42, v245, v241, v62\n"
    "v_mfma_f32_16x16x32_bf16 a[136:139], v[36:39], a[24:27], a[136:139]\n"
    "v_perm_b32 v43, v253, v249, v62\n"
    "v_mfma_f32_16x16x32_bf16 a[140:143], v[36:39], a[28:31], a[140:143]\n"
    "v_perm_b32 v44, v229, v225, v63\n"
    "v_mfma_f32_16x16x32_bf16 v[80:83], v[40:43], a[16:19], v[80:83]\n"
    "v_perm_b32 v45, v237, v233, v63\n"
    "v_mfma_f32_16x16x32_bf16 v[84:87], v[40:43], a[20:23], v[84:87]\n"
    "v_perm_b32 v46, v245, v241, v63\n"
    "v_mfma_f32_16x16x32_bf16 a[144:147], v[40:43], a[24:27], a[144:147]\n"
    "v_perm_b32 v47, v253, v249, v63\n"
    "v_mfma_f32_16x16x32_bf16 a[148:151], v[40:43], a[28:31], a[148:151]\n"
    "v_perm_b32 v32, v230, v226, v62\n"
    "v_mfma_f32_16x16x32_bf16 v[88:91], v[44:47], a[16:19], v[88:91]\n"
    "v_perm_b32 v33, v238, v234, v62\n"
    "v_mfma_f32_16x16x32_bf16 v[92:95], v[44:47], a[20:23], v[92:95]\n"
    "v_perm_b32 v34, v246, v242, v62\n"
    "v_mfma_f32_16x16x32_bf16 a[152:155], v[44:47], a[24:27], a[152:155]\n"
    "v_perm_b32 v35, v254, v250, v62\n"
    "v_mfma_f32_16x16x32_bf16 a[156:159], v[44:47], a[28:31], a[156:159]\n"
    "v_perm_b32 v36, v230, v226, v63\n"
    "v_mfma_f32_16x16x32_bf16 v[96:99], v[32:35], a[16:19], v[96:99]\n"
    "v_perm_b32 v37, v238, v234, v63\n"
    "v_mfma_f32_16x16x32_bf16 v[100:103], v[32:35], a[20:23], v[100:103]\n"
    "v_perm_b32 v38, v246, v242, v63\n"
    "v_mfma_f32_16x16x32_bf16 a[160:163], v[32:35], a[24:27], a[160:163]\n"
    "v_perm_b32 v39, v254, v250, v63\n"
    "v_mfma_f32_16x16x32_bf16 a[164:167], v[32:35], a[28:31], a[164:167]\n"
    "v_perm_b32 v40, v231, v227, v62\n"
    "v_mfma_f32_16x16x32_bf16 v[104:107], v[36:39], a[16:19], v[104:107]\n"
    "v_perm_b32 v41, v239, v235, v62\n"
    "v_mfma_f32_16x16x32_bf16 v[108:111], v[36:39], a[20:23], v[108:111]\n"
    "v_perm_b32 v42, v247, v243, v62\n"
    "v_mfma_f32_16x16x32_bf16 a[168:171], v[36:39], a[24:27], a[168:171]\n"
    "v_perm_b32 v43, v255, v251, v62\n"
    "v_mfma_f32_16x16x32_bf16 a[172:175], v[36:39], a[28:31], a[172:175]\n"
    "v_perm_b32 v44, v231, v227, v63\n"
    "v_mfma_f32_16x16x32_bf16 v[112:115], v[40:43], a[16:19], v[112:115]\n"
    "v_perm_b32 v45, v239, v235, v63\n"
    "v_mfma_f32_16x16x32_bf16 v[116:119], v[40:43], a[20:23], v[116:119]\n"
    "v_perm_b32 v46, v247, v243, v63\n"
    "v_mfma_f32_16x16x32_bf16 a[176:179], v[40:43], a[24:27], a[176:179]\n"
    "v_perm_b32 v47, v255, v251, v63\n"
    "v_mfma_f32_16x16x32_bf16 a[180:183], v[40:43], a[28:31], a[180:183]\n"
    "s_nop 0\n"
    "v_mfma_f32_16x16x32_bf16 v[120:123], v[44:47], a[16:19], v[120:123]\n"
    "v_mfma_f32_16x16x32_bf16 v[124:127], v[44:47], a[20:23], v[124:127]\n"
    "v_mfma_f32_16x16x32_bf16 a[184:187], v[44:47], a[24:27], a[184:187]\n"
    "v_mfma_f32_16x16x32_bf16 a[188:191], v[44:47], a[28:31], a[188:191]\n"
    "s_cmp_eq_u32 s87, 0\n"
    "s_cbranch_scc0 Lfin_%=\n"
    "v_pk_mul_f32 v[64:65], v[64:65], s[74:75]\n"
    "v_pk_mul_f32 v[66:67], v[66:67], s[74:75]\n"
    "v_pk_mul_f32 v[68:69], v[68:69], s[74:75]\n"
    "v_pk_mul_f32 v[70:71], v[70:71], s[74:75]\n"
    "v_pk_mul_f32 v[72:73], v[72:73], s[74:75]\n"
    "v_pk_mul_f32 v[74:75], v[74:75], s[74:75]\n"
    "v_pk_mul_f32 v[76:77], v[76:77], s[74:75]\n"
    "v_pk_mul_f32 v[78:79], v[78:79], s[74:75]\n"
    "v_pk_mul_f32 v[80:81], v[80:81], s[74:75]\n"
    "v_pk_mul_f32 v[82:83], v[82:83], s[74:75]\n"
    "v_pk_mul_f32 v[84:85], v[84:85], s[74:75]\n"
    "v_pk_mul_f32 v[86:87], v[86:87], s[74:75]\n"
    "v_pk_mul_f32 v[88:89], v[88:89], s[74:75]\n"
    "v_pk_mul_f32 v[90:91], v[90:91], s[74:75]\n"
    "v_pk_mul_f32 v[92:93], v[92:93], s[74:75]\n"
    "v_pk_mul_f32 v[94:95], v[94:95], s[74:75]\n"
    "v_pk_mul_f32 v[96:97], v[96:97], s[74:75]\n"
    "v_pk_mul_f32 v[98:99], v[98:99], s[74:75]\n"
    "v_pk_mul_f32 v[100:101], v[100:101], s[74:75]\n"
    "v_pk_mul_f32 v[102:103], v[102:103], s[74:75]\n"
    "v_pk_mul_f32 v[104:105], v[104:105], s[74:75]\n"
    "v_pk_mul_f32 v[106:107], v[106:107], s[74:75]\n"
    "v_pk_mul_f32 v[108:109], v[108:109], s[74:75]\n"
    "v_pk_mul_f32 v[110:111], v[110:111], s[74:75]\n"
    "v_pk_mul_f32 v[112:113], v[112:113], s[74:75]\n"
    "v_pk_mul_f32 v[114:115], v[114:115], s[74:75]\n"
    "v_pk_mul_f32 v[116:117], v[116:117], s[74:75]\n"
    "v_pk_mul_f32 v[118:119], v[118:119], s[74:75]\n"
    "v_pk_mul_f32 v[120:121], v[120:121], s[74:75]\n"
    "v_pk_mul_f32 v[122:123], v[122:123], s[74:75]\n"
    "v_pk_mul_f32 v[124:125], v[124:125], s[74:75]\n"
    "v_pk_mul_f32 v[126:127], v[126:127], s[74:75]\n"
    "v_cvt_pk_bf16_f32 v32, v64, v72\n"
    "v_cvt_pk_bf16_f32 v48, v68, v76\n"
    "v_cvt_pk_bf16_f32 v33, v80, v88\n"
    "v_cvt_pk_bf16_f32 v49, v84, v92\n"
    "v_cvt_pk_bf16_f32 v34, v96, v104\n"
    "v_cvt_pk_bf16_f32 v50, v100, v108\n"
    "v_cvt_pk_bf16_f32 v35, v112, v120\n"
    "v_cvt_pk_bf16_f32 v51, v116, v124\n"
    "v_cvt_pk_bf16_f32 v36, v65, v73\n"
    "v_cvt_pk_bf16_f32 v52, v69, v77\n"
    "v_cvt_pk_bf16_f32 v37, v81, v89\n"
    "v_cvt_pk_bf16_f32 v53, v85, v93\n"
    "v_cvt_pk_bf16_f32 v38, v97, v105\n"
    "v_cvt_pk_bf16_f32 v54, v101, v109\n"
    "v_cvt_pk_bf16_f32 v39, v113, v121\n"
    "v_cvt_pk_bf16_f32 v55, v117, v125\n"
    "v_cvt_pk_bf16_f32 v40, v66, v74\n"
    "v_cvt_pk_bf16_f32 v56, v70, v78\n"
    "v_cvt_pk_bf16_f32 v41, v82, v90\n"
    "v_cvt_pk_bf16_f32 v57, v86, v94\n"
    "v_cvt_pk_bf16_f32 v42, v98, v106\n"
    "v_cvt_pk_bf16_f32 v58, v102, v110\n"
    "v_cvt_pk_bf16_f32 v43, v114, v122\n"
    "v_cvt_pk_bf16_f32 v59, v118, v126\n"
    "v_cvt_pk_bf16_f32 v44, v67, v75\n"
    "v_cvt_pk_bf16_f32 v60, v71, v79\n"
    "v_cvt_pk_bf16_f32 v45, v83, v91\n"
    "v_cvt_pk_bf16_f32 v61, v87, v95\n"
    "v_cvt_pk_bf16_f32 v46, v99, v107\n"
    "v_cvt_pk_bf16_f32 v62, v103, v111\n"
    "v_cvt_pk_bf16_f32 v47, v115, v123\n"
    "v_cvt_pk_bf16_f32 v63, v119, v127\n"
    "v_permlane16_swap_b32 v32, v48\n"
    "v_permlane16_swap_b32 v33, v49\n"
    "v_permlane16_swap_b32 v34, v50\n"
    "v_permlane16_swap_b32 v35, v51\n"
    "v_permlane16_swap_b32 v36, v52\n"
    "v_permlane16_swap_b32 v37, v53\n"
    "v_permlane16_swap_b32 v38, v54\n"
    "v_permlane16_swap_b32 v39, v55\n"
    "v_permlane16_swap_b32 v40, v56\n"
    "v_permlane16_swap_b32 v41, v57\n"
    "v_permlane16_swap_b32 v42, v58\n"
    "v_permlane16_swap_b32 v43, v59\n"
    "v_permlane16_swap_b32 v44, v60\n"
    "v_permlane16_swap_b32 v45, v61\n"
    "v_permlane16_swap_b32 v46, v62\n"
    "v_permlane16_swap_b32 v47, v63\n"
    "s_waitcnt vmcnt(20) lgkmcnt(0)\n"
    "s_barrier\n"
    "s_add_u32 s82, s35, 49152\n"
    "s_and_b32 s82, s82, 0x1c000\n"
    "s_add_u32 s82, s82, s89\n"
    "s_sub_u32 s88, s82, s89\n"
    "ds_read_b128 a[0:3], v8 offset:0\n"
    "ds_read_b128 a[4:7], v9 offset:0\n"
    "ds_read_b128 a[8:11], v10 offset:0\n"
    "ds_read_b128 a[12:15], v11 offset:0\n"
    "ds_read_b128 a[16:19], v12 offset:0\n"
    "ds_read_b128 a[20:23], v13 offset:0\n"
    "ds_read_b128 a[24:27], v14 offset:0\n"
    "ds_read_b128 a[28:31], v15 offset:0\n"
    "ds_read_b128 a[32:35], v8 offset:8192\n"
    "ds_read_b128 a[36:39], v9 offset:8192\n"
    "ds_read_b128 a[40:43], v10 offset:8192\n"
    "ds_read_b128 a[44:47], v11 offset:8192\n"
    "ds_read_b128 a[48:51], v12 offset:8192\n"
    "ds_read_b128 a[52:55], v13 offset:8192\n"
    "ds_read_b128 a[56:59], v14 offset:8192\n"
    "ds_read_b128 a[60:63], v15 offset:8192\n"
    "s_add_u32 s93, s93, 49152\n"
    "s_lshl_b32 s94, s64, 19\n"
    "s_lshl_b32 s78, s68, 8\n"
    "s_add_u32 s94, s94, s78\n"
    "s_mov_b32 s98, 8190\n"
    "s_mov_b32 s99, 4079\n"
    "s_mov_b32 s95, 16777216\n"
    "Lunit_%=:\n"
    "s_waitcnt lgkmcnt(0)\n"
    "v_mfma_f32_32x32x16_bf16 v[128:143], a[0:3], v[32:35], 0\n"
    "ds_read_b128 a[0:3], v8 offset:16384\n"
    "v_mov_b32 v64, 0\n"
    "v_mov_b32 v65, 0\n"
    "v_mov_b32 v66, 0\n"
    "v_mov_b32 v67, 0\n"
    "v_mov_b32 v68, 0\n"
    "v_mfma_f32_32x32x16_bf16 v[128:143], a[4:7], v[36:39], v[128:143]\n"
    "ds_read_b128 a[4:7], v9 offset:16384\n"
    "v_mov_b32 v69, 0\n"
    "v_mov_b32 v70, 0\n"
    "v_mov_b32 v71, 0\n"
    "v_mov_b32 v72, 0\n"
    "v_mfma_f32_32x32x16_bf16 v[128:143], a[8:11], v[40:43], v[128:143]\n"
    "ds_read_b128 a[8:11], v10 offset:16384\n"
    "v_mov_b32 v73, 0\n"
    "v_mov_b32 v74, 0\n"
    "v_mov_b32 v75, 0\n"
    "v_mov_b32 v76, 0\n"
    "v_mfma_f32_32x32x16_bf16 v[128:143], a[12:15], v[44:47], v[128:143]\n"
    "ds_read_b128 a[12:15], v11 offset:16384\n"
    "v_mov_b32 v77, 0\n"
    "v_mov_b32 v78, 0\n"
    "v_mov_b32 v79, 0\n"
    "v_mov_b32 v80, 0\n"
    "v_mfma_f32_32x32x16_bf16 v[128:143], a[16:19], v[48:51], v[128:143]\n"
    "ds_read_b128 a[16:19], v12 offset:16384\n"
    "v_mov_b32 v81, 0\n"
    "v_mov_b32 v82, 0\n"
    "v_mov_b32 v83, 0\n"
    "v_mov_b32 v84, 0\n"
    "v_mfma_f32_32x32x16_bf16 v[128:143], a[20:23], v[52:55], v[128:143]\n"
    "ds_read_b128 a[20:23], v13 offset:16384\n"
    "v_mov_b32 v85, 0\n"
    "v_mov_b32 v86, 0\n"
    "v_mov_b32 v87, 0\n"
    "v_mov_b32 v88, 0\n"
    "v_mfma_f32_32x32x16_bf16 v[128:143], a[24:27], v[56:59], v[128:143]\n"
    "ds_read_b128 a[24:27], v14 offset:16384\n"
    "v_mov_b32 v89, 0\n"
    "v_mov_b32 v90, 0\n"
    "v_mov_b32 v91, 0\n"
    "v_mov_b32 v92, 0\n"
    "v_mfma_f32_32x32x16_bf16 v[128:143], a[28:31], v[60:63], v[128:143]\n"
    "ds_read_b128 a[28:31], v15 offset:16384\n"
    "v_mov_b32 v93, 0\n"
    "v_mov_b32 v94, 0\n"
    "v_mov_b32 v95, 0\n"
    "v_mov_b32 v96, 0\n"
    "v_mfma_f32_32x32x16_bf16 v[144:159], a[32:35], v[32:35], 0\n"
    "ds_read_b128 a[32:35], v8 offset:24576\n"
    "v_mov_b32 v97, 0\n"
    "v_mov_b32 v98, 0\n"
    "v_mov_b32 v99, 0\n"
    "v_mov_b32 v100, 0\n"
    "v_mfma_f32_32x32x16_bf16 v[144:159], a[36:39], v[36:39], v[144:159]\n"
    "ds_read_b128 a[36:39], v9 offset:24576\n"
    "v_mov_b32 v101, 0\n"
    "v_mov_b32 v102, 0\n"
    "v_mov_b32 v103, 0\n"
    "v_mov_b32 v104, 0\n"
    "v_mfma_f32_32x32x16_bf16 v[144:159], a[40:43], v[40:43], v[144:159]\n"
    "ds_read_b128 a[40:43], v10 offset:24576\n"
    "v_mov_b32 v105, 0\n"
    "v_mov_b32 v106, 0\n"
    "v_mov_b32 v107, 0\n"
    "v_mov_b32 v108, 0\n"
    "v_mfma_f32_32x32x16_bf16 v[144:159], a[44:47], v[44:47], v[144:159]\n"
    "ds_read_b128 a[44:47], v11 offset:24576\n"
    "v_mov_b32 v109, 0\n"
    "v_mov_b32 v110, 0\n"
    "v_mov_b32 v111, 0\n"
    "v_mov_b32 v112, 0\n"
    "v_mfma_f32_32x32x16_bf16 v[144:159], a[48:51], v[48:51], v[144:159]\n"
    "ds_read_b128 a[48:51], v12 offset:24576\n"
    "v_mov_b32 v113, 0\n"
    "v_mov_b32 v114, 0\n"
    "v_mov_b32 v115, 0\n"
    "v_mov_b32 v116, 0\n"
    "v_mfma_f32_32x32x16_bf16 v[144:159], a[52:55], v[52:55], v[144:159]\n"
    "ds_read_b128 a[52:55], v13 offset:24576\n"
    "v_mov_b32 v117, 0\n"
    "v_mov_b32 v118, 0\n"
    "v_mov_b32 v119, 0\n"
    "v_mov_b32 v120, 0\n"
    "v_mfma_f32_32x32x16_bf16 v[144:159], a[56:59], v[56:59], v[144:159]\n"
    "ds_read_b128 a[56:59], v14 offset:24576\n"
    "v_mov_b32 v121, 0\n"
    "v_mov_b32 v122, 0\n"
    "v_mov_b32 v123, 0\n"
    "v_mov_b32 v124, 0\n"
    "v_mfma_f32_32x32x16_bf16 v[144:159], a[60:63], v[60:63], v[144:159]\n"
    "ds_read_b128 a[60:63], v15 offset:24576\n"
    "v_mov_b32 v125, 0\n"
    "v_mov_b32 v126, 0\n"
    "v_mov_b32 v127, 0\n"
    "v_mov_b32 v25, 0\n"
    "v_max3_f32 v26, v128, v129, v130\n"
    "v_max3_f32 v28, v131, v132, v133\n"
    "v_max3_f32 v26, v26, v134, v135\n"
    "v_max3_f32 v28, v28, v136, v137\n"
    "v_max3_f32 v26, v26, v138, v139\n"
    "v_max3_f32 v28, v28, v140, v141\n"
    "v_max3_f32 v26, v26, v142, v143\n"
    "v_max3_f32 v28, v28, v144, v145\n"
    "v_max3_f32 v26, v26, v146, v147\n"
    "v_max3_f32 v28, v28, v148, v149\n"
    "v_max3_f32 v26, v26, v150, v151\n"
    "v_max3_f32 v28, v28, v152, v153\n"
    "v_max3_f32 v26, v26, v154, v155\n"
    "v_max3_f32 v28, v28, v156, v157\n"
    "v_max3_f32 v26, v26, v158, v159\n"
    "v_max_f32 v26, v26, v28\n"
    "v_mov_b32 v28, v26\n"
    "s_nop 1\n"
    "v_permlane32_swap_b32 v26, v28\n"
    "v_max_f32 v26, v26, v28\n"
    "v_pk_add_f32 v[128:129], v[128:129], v[26:27] op_sel_hi:[1,0] neg_lo:[0,1] neg_hi:[0,1]\n"
    "v_pk_add_f32 v[130:131], v[130:131], v[26:27] op_sel_hi:[1,0] neg_lo:[0,1] neg_hi:[0,1]\n"
    "v_pk_add_f32 v[132:133], v[132:133], v[26:27] op_sel_hi:[1,0] neg_lo:[0,1] neg_hi:[0,1]\n"
    "v_pk_add_f32 v[134:135], v[134:135], v[26:27] op_sel_hi:[1,0] neg_lo:[0,1] neg_hi:[0,1]\n"
    "v_pk_add_f32 v[136:137], v[136:137], v[26:27] op_sel_hi:[1,0] neg_lo:[0,1] neg_hi:[0,1]\n"
    "v_pk_add_f32 v[138:139], v[138:139], v[26:27] op_sel_hi:[1,0] neg_lo:[0,1] neg_hi:[0,1]\n"
    "v_pk_add_f32 v[140:141], v[140:141], v[26:27] op_sel_hi:[1,0] neg_lo:[0,1] neg_hi:[0,1]\n"
    "v_pk_add_f32 v[142:143], v[142:143], v[26:27] op_sel_hi:[1,0] neg_lo:[0,1] neg_hi:[0,1]\n"
    "v_pk_add_f32 v[144:145], v[144:145], v[26:27] op_sel_hi:[1,0] neg_lo:[0,1] neg_hi:[0,1]\n"
    "v_pk_add_f32 v[146:147], v[146:147], v[26:27] op_sel_hi:[1,0] neg_lo:[0,1] neg_hi:[0,1]\n"
    "v_pk_add_f32 v[148:149], v[148:149], v[26:27] op_sel_hi:[1,0] neg_lo:[0,1] neg_hi:[0,1]\n"
    "v_pk_add_f32 v[150:151], v[150:151], v[26:27] op_sel_hi:[1,0] neg_lo:[0,1] neg_hi:[0,1]\n"
    "v_pk_add_f32 v[152:153], v[152:153], v[26:27] op_sel_hi:[1,0] neg_lo:[0,1] neg_hi:[0,1]\n"
    "v_pk_add_f32 v[154:155], v[154:155], v[26:27] op_sel_hi:[1,0] neg_lo:[0,1] neg_hi:[0,1]\n"
    "v_pk_add_f32 v[156:157], v[156:157], v[26:27] op_sel_hi:[1,0] neg_lo:[0,1] neg_hi:[0,1]\n"
    "v_pk_add_f32 v[158:159], v[158:159], v[26:27] op_sel_hi:[1,0] neg_lo:[0,1] neg_hi:[0,1]\n"
    "v_sub_f32 v29, 0, v26\n"
    "v_sub_f32 v160, 0, v26\n"
    "v_sub_f32 v161, 0, v26\n"
    "v_sub_f32 v162, 0, v26\n"
    "v_sub_f32 v163, 0, v26\n"
    "v_sub_f32 v164, 0, v26\n"
    "v_sub_f32 v165, 0, v26\n"
    "v_sub_f32 v166, 0, v26\n"
    "v_sub_f32 v167, 0, v26\n"
    "v_sub_f32 v168, 0, v26\n"
    "v_sub_f32 v169, 0, v26\n"
    "v_sub_f32 v170, 0, v26\n"
    "v_sub_f32 v171, 0, v26\n"
    "v_sub_f32 v172, 0, v26\n"
    "v_sub_f32 v173, 0, v26\n"
    "v_sub_f32 v174, 0, v26\n"
    "v_sub_f32 v175, 0, v26\n"
    "v_exp_f32 v128, v128\n"
    "v_exp_f32 v129, v129\n"
    "v_exp_f32 v130, v130\n"
    "v_exp_f32 v131, v131\n"
    "v_exp_f32 v132, v132\n"
    "v_add_f32 v26, v128, v130\n"
    "v_exp_f32 v133, v133\n"
    "v_add_f32 v28, v129, v131\n"
    "v_cvt_pk_bf16_f32 v128, v128, v129\n"
    "v_cvt_pk_bf16_f32 v129, v130, v131\n"
    "v_exp_f32 v134, v134\n"
    "v_add_f32 v26, v26, v132\n"
    "v_exp_f32 v135, v135\n"
    "v_add_f32 v28, v28, v133\n"
    "v_cvt_pk_bf16_f32 v130, v132, v133\n"
    "v_exp_f32 v136, v136\n"
    "v_add_f32 v26, v26, v134\n"
    "v_exp_f32 v137, v137\n"
    "v_add_f32 v28, v28, v135\n"
    "v_cvt_pk_bf16_f32 v131, v134, v135\n"
    "v_exp_f32 v138, v138\n"
    "v_add_f32 v26, v26, v136\n"
    "v_exp_f32 v139, v139\n"
    "v_add_f32 v28, v28, v137\n"
    "v_cvt_pk_bf16_f32 v136, v136, v137\n"
    "v_exp_f32 v140, v140\n"
    "v_add_f32 v26, v26, v138\n"
    "v_exp_f32 v141, v141\n"
    "v_add_f32 v28, v28, v139\n"
    "v_cvt_pk_bf16_f32 v137, v138, v139\n"
    "v_exp_f32 v142, v142\n"
    "v_add_f32 v26, v26, v140\n"
    "v_exp_f32 v143, v143\n"
    "v_add_f32 v28, v28, v141\n"
    "v_cvt_pk_bf16_f32 v138, v140, v141\n"
    "v_add_f32 v26, v26, v142\n"
    "v_add_f32 v28, v28, v143\n"
    "v_cvt_pk_bf16_f32 v139, v142, v143\n"
    "s_mov_b32 m0, s93\n"
    "s_bitcmp1_b32 s98, s69\n"
    "s_memrealtime s[74:75]\n"
    "s_waitcnt lgkmcnt(0)\n"
    "s_add_u32 s78, s74, 0\n"
    "s_add_u32 s79, s34, 105\n"
    "s_cmp_eq_u32 s87, 0\n"
    "s_cselect_b32 s34, s78, s78\n"
    "s_mov_b32 s84, 109\n"
    "s_cselect_b32 s84, 105, s84\n"
    "s_add_u32 s84, s84, s77\n"
    "s_branch La_%=\n"
    "Lb_%=:\n"
    "s_waitcnt vmcnt(8) lgkmcnt(0)\n"
    "s_sub_i32 s78, s34, s74\n"
    "s_cmp_gt_i32 s78, 0\n"
    "s_cbranch_scc1 Lpaceb_%=\n"
    "Lpacebr_%=:\n"
    "s_barrier\n"
    "v_mfma_f32_32x32x16_bf16 v[128:143], a[0:3], v[32:35], v[160:175]\n"
    "ds_read_b64_tr_b16 a[64:65], v16 offset:16384\n"
    "ds_read_b64_tr_b16 a[66:67], v16 offset:18432\n"
    "s_bitcmp1_b32 s98, s69\n"
    "v_exp_f32 v240, v240\n"
    "v_exp_f32 v241, v241\n"
    "v_mfma_f32_32x32x16_bf16 v[128:143], a[4:7], v[36:39], v[128:143]\n"
    "s_cbranch_scc0 LaltbV_%=\n"
    "buffer_load_dwordx4 v24, s[44:47], s82 offen offset:0 nt lds\n"
    "buffer_load_dwordx4 v24, s[44:47], s82 offen offset:1024 nt lds\n"
    "buffer_load_dwordx4 v24, s[44:47], s82 offen offset:2048 nt lds\n"
    "buffer_load_dwordx4 v24, s[44:47], s82 offen offset:3072 nt lds\n"
    "LbV_%=:\n"
    "v_mfma_f32_32x32x16_bf16 v[128:143], a[8:11], v[40:43], v[128:143]\n"
    "s_add_u32 s88, s88, 16384\n"
    "s_and_b32 s88, s88, 0x1c000\n"
    "v_exp_f32 v242, v242\n"
    "v_add_f32 v26, v26, v240\n"
    "v_exp_f32 v243, v243\n"
    "v_mfma_f32_32x32x16_bf16 v[128:143], a[12:15], v[44:47], v[128:143]\n"
    "ds_read_b64_tr_b16 a[68:69], v17 offset:16384\n"
    "s_add_u32 s82, s89, s88\n"
    "s_add_u32 s93, s92, 65536\n"
    "v_add_f32 v28, v28, v241\n"
    "v_cvt_pk_bf16_f32 v240, v240, v241\n"
    "v_exp_f32 v244, v244\n"
    "v_mfma_f32_32x32x16_bf16 v[128:143], a[16:19], v[48:51], v[128:143]\n"
    "ds_read_b64_tr_b16 a[70:71], v17 offset:18432\n"
    "s_mov_b32 m0, s92\n"
    "s_bitcmp1_b32 s99, s69\n"
    "v_add_f32 v26, v26, v242\n"
    "v_exp_f32 v245, v245\n"
    "v_mfma_f32_32x32x16_bf16 v[128:143], a[20:23], v[52:55], v[128:143]\n"
    "s_cbranch_scc0 LaltbK_%=\n"
    "buffer_load_dwordx4 v20, s[40:43], s82 offen offset:0 nt lds\n"
    "buffer_load_dwordx4 v21, s[40:43], s82 offen offset:1024 nt lds\n"
    "buffer_load_dwordx4 v22, s[40:43], s82 offen offset:2048 nt lds\n"
    "buffer_load_dwordx4 v23, s[40:43], s82 offen offset:3072 nt lds\n"
    "LbK_%=:\n"
    "v_mfma_f32_32x32x16_bf16 v[128:143], a[24:27], v[56:59], v[128:143]\n"
    "ds_read_b64_tr_b16 a[72:73], v18 offset:16384\n"
    "ds_read_b64_tr_b16 a[74:75], v18 offset:18432\n"
    "v_add_f32 v28, v28, v243\n"
    "v_cvt_pk_bf16_f32 v241, v242, v243\n"
    "v_exp_f32 v246, v246\n"
    "v_mfma_f32_32x32x16_bf16 v[128:143], a[28:31], v[60:63], v[128:143]\n"
    "ds_read_b64_tr_b16 a[76:77], v19 offset:16384\n"
    "v_add_f32 v26, v26, v244\n"
    "v_exp_f32 v247, v247\n"
    "v_add_f32 v28, v28, v245\n"
    "v_cvt_pk_bf16_f32 v242, v244, v245\n"
    "v_mfma_f32_32x32x16_bf16 v[144:159], a[32:35], v[32:35], v[160:175]\n"
    "ds_read_b64_tr_b16 a[78:79], v19 offset:18432\n"
    "v_exp_f32 v248, v248\n"
    "v_add_f32 v26, v26, v246\n"
    "v_exp_f32 v249, v249\n"
    "v_mfma_f32_32x32x16_bf16 v[144:159], a[36:39], v[36:39], v[144:159]\n"
    "ds_read_b64_tr_b16 a[80:81], v16 offset:20480\n"
    "v_add_f32 v28, v28, v247\n"
    "v_cvt_pk_bf16_f32 v243, v246, v247\n"
    "v_exp_f32 v250, v250\n"
    "v_add_f32 v26, v26, v248\n"
    "v_mfma_f32_32x32x16_bf16 v[144:159], a[40:43], v[40:43], v[144:159]\n"
    "ds_read_b64_tr_b16 a[82:83], v16 offset:22528\n"
    "ds_read_b64_tr_b16 a[84:85], v17 offset:20480\n"
    "s_add_u32 s34, s74, s84\n"
    "v_exp_f32 v251, v251\n"
    "v_add_f32 v28, v28, v249\n"
    "v_mfma_f32_32x32x16_bf16 v[144:159], a[44:47], v[44:47], v[144:159]\n"
    "ds_read_b64_tr_b16 a[86:87], v17 offset:22528\n"
    "v_cvt_pk_bf16_f32 v248, v248, v249\n"
    "v_exp_f32 v252, v252\n"
    "v_add_f32 v26, v26, v250\n"
    "v_mfma_f32_32x32x16_bf16 v[144:159], a[48:51], v[48:51], v[144:159]\n"
    "ds_read_b64_tr_b16 a[88:89], v18 offset:20480\n"
    "v_exp_f32 v253, v253\n"
    "v_add_f32 v28, v28, v251\n"
    "v_cvt_pk_bf16_f32 v249, v250, v251\n"
    "v_mfma_f32_32x32x16_bf16 v[144:159], a[52:55], v[52:55], v[144:159]\n"
    "ds_read_b64_tr_b16 a[90:91], v18 offset:22528\n"
    "v_exp_f32 v254, v254\n"
    "v_add_f32 v26, v26, v252\n"
    "v_exp_f32 v255, v255\n"
    "v_add_f32 v28, v28, v253\n"
    "v_mfma_f32_32x32x16_bf16 v[144:159], a[56:59], v[56:59], v[144:159]\n"
    "ds_read_b64_tr_b16 a[92:93], v19 offset:20480\n"
    "v_cvt_pk_bf16_f32 v250, v252, v253\n"
    "v_add_f32 v26, v26, v254\n"
    "v_add_f32 v28, v28, v255\n"
    "v_cvt_pk_bf16_f32 v251, v254, v255\n"
    "v_add_f32 v26, v26, v28\n"
    "v_mfma_f32_32x32x16_bf16 v[144:159], a[60:63], v[60:63], v[144:159]\n"
    "ds_read_b64_tr_b16 a[94:95], v19 offset:22528\n"
    "v_cmp_nge_f32 vcc, s76, v26\n"
    "s_nop 0\n"
    "s_cbranch_vccnz Lrxb_%=\n"
    "Lrxbr_%=:\n"
    "v_add_f32 v25, v25, v26\n"
    "s_waitcnt lgkmcnt(8)\n"
    "v_mfma_f32_32x32x16_bf16 v[64:79], a[64:67], v[224:227], v[64:79]\n"
    "ds_read_b64_tr_b16 a[96:97], v16 offset:24576\n"
    "ds_read_b64_tr_b16 a[98:99], v16 offset:26624\n"
    "ds_read_b64_tr_b16 a[100:101], v17 offset:24576\n"
    "ds_read_b64_tr_b16 a[102:103], v17 offset:26624\n"
    "v_mfma_f32_32x32x16_bf16 v[80:95], a[68:71], v[224:227], v[80:95]\n"
    "ds_read_b64_tr_b16 a[104:105], v18 offset:24576\n"
    "ds_read_b64_tr_b16 a[106:107], v18 offset:26624\n"
    "v_exp_f32 v128, v128\n"
    "v_exp_f32 v129, v129\n"
    "v_mfma_f32_32x32x16_bf16 v[96:111], a[72:75], v[224:227], v[96:111]\n"
    "ds_read_b128 a[0:3], v8 offset:16384\n"
    "ds_read_b64_tr_b16 a[108:109], v19 offset:24576\n"
    "ds_read_b64_tr_b16 a[110:111], v19 offset:26624\n"
    "v_exp_f32 v130, v130\n"
    "v_mfma_f32_32x32x16_bf16 v[112:127], a[76:79], v[224:227], v[112:127]\n"
    "ds_read_b128 a[4:7], v9 offset:16384\n"
    "ds_read_b64_tr_b16 a[112:113], v16 offset:28672\n"
    "ds_read_b64_tr_b16 a[114:115], v16 offset:30720\n"
    "v_exp_f32 v131, v131\n"
    "v_exp_f32 v132, v132\n"
    "s_waitcnt lgkmcnt(12)\n"
    "v_mfma_f32_32x32x16_bf16 v[64:79], a[80:83], v[232:235], v[64:79]\n"
    "ds_read_b128 a[8:11], v10 offset:16384\n"
    "ds_read_b64_tr_b16 a[116:117], v17 offset:28672\n"
    "ds_read_b64_tr_b16 a[118:119], v17 offset:30720\n"
    "v_add_f32 v26, v128, v130\n"
    "v_exp_f32 v133, v133\n"
    "v_mfma_f32_32x32x16_bf16 v[80:95], a[84:87], v[232:235], v[80:95]\n"
    "ds_read_b64_tr_b16 a[120:121], v18 offset:28672\n"
    "ds_read_b64_tr_b16 a[122:123], v18 offset:30720\n"
    "ds_read_b64_tr_b16 a[124:125], v19 offset:28672\n"
    "ds_read_b64_tr_b16 a[126:127], v19 offset:30720\n"
    "ds_read_b128 a[12:15], v11 offset:16384\n"
    "v_add_f32 v28, v129, v131\n"
    "v_mfma_f32_32x32x16_bf16 v[96:111], a[88:91], v[232:235], v[96:111]\n"
    "ds_read_b128 a[16:19], v12 offset:16384\n"
    "ds_read_b128 a[20:23], v13 offset:16384\n"
    "v_cvt_pk_bf16_f32 v128, v128, v129\n"
    "v_cvt_pk_bf16_f32 v129, v130, v131\n"
    "v_exp_f32 v134, v134\n"
    "v_mfma_f32_32x32x16_bf16 v[112:127], a[92:95], v[232:235], v[112:127]\n"
    "ds_read_b128 a[24:27], v14 offset:16384\n"
    "v_add_f32 v26, v26, v132\n"
    "v_exp_f32 v135, v135\n"
    "v_add_f32 v28, v28, v133\n"
    "v_cvt_pk_bf16_f32 v130, v132, v133\n"
    "s_waitcnt lgkmcnt(14)\n"
    "v_mfma_f32_32x32x16_bf16 v[64:79], a[96:99], v[240:243], v[64:79]\n"
    "ds_read_b128 a[28:31], v15 offset:16384\n"
    "ds_read_b128 a[32:35], v8 offset:24576\n"
    "v_exp_f32 v136, v136\n"
    "v_add_f32 v26, v26, v134\n"
    "v_mfma_f32_32x32x16_bf16 v[80:95], a[100:103], v[240:243], v[80:95]\n"
    "ds_read_b128 a[36:39], v9 offset:24576\n"
    "v_exp_f32 v137, v137\n"
    "v_add_f32 v28, v28, v135\n"
    "v_xor_b32 v16, 0x8000, v16\n"
    "v_cvt_pk_bf16_f32 v131, v134, v135\n"
    "v_mfma_f32_32x32x16_bf16 v[96:111], a[104:107], v[240:243], v[96:111]\n"
    "ds_read_b128 a[40:43], v10 offset:24576\n"
    "s_add_u32 s92, s92, 16384\n"
    "v_exp_f32 v138, v138\n"
    "v_add_f32 v26, v26, v136\n"
    "v_exp_f32 v139, v139\n"
    "v_mfma_f32_32x32x16_bf16 v[112:127], a[108:111], v[240:243], v[112:127]\n"
    "ds_read_b128 a[44:47], v11 offset:24576\n"
    "ds_read_b128 a[48:51], v12 offset:24576\n"
    "s_and_b32 s92, s92, 0xffff\n"
    "v_add_f32 v28, v28, v137\n"
    "v_xor_b32 v17, 0x8000, v17\n"
    "v_cvt_pk_bf16_f32 v136, v136, v137\n"
    "s_waitcnt lgkmcnt(10)\n"
    "v_mfma_f32_32x32x16_bf16 v[64:79], a[112:115], v[248:251], v[64:79]\n"
    "ds_read_b128 a[52:55], v13 offset:24576\n"
    "s_mov_b32 m0, s93\n"
    "v_exp_f32 v140, v140\n"
    "v_add_f32 v26, v26, v138\n"
    "v_mfma_f32_32x32x16_bf16 v[80:95], a[116:119], v[248:251], v[80:95]\n"
    "ds_read_b128 a[56:59], v14 offset:24576\n"
    "ds_read_b128 a[60:63], v15 offset:24576\n"
    "s_add_u32 s69, s69, 1\n"
    "v_exp_f32 v141, v141\n"
    "v_xor_b32 v18, 0x8000, v18\n"
    "v_add_f32 v28, v28, v139\n"
    "v_mfma_f32_32x32x16_bf16 v[96:111], a[120:123], v[248:251], v[96:111]\n"
    "v_cvt_pk_bf16_f32 v137, v138, v139\n"
    "v_exp_f32 v142, v142\n"
    "v_add_f32 v26, v26, v140\n"
    "v_exp_f32 v143, v143\n"
    "v_mfma_f32_32x32x16_bf16 v[112:127], a[124:127], v[248:251], v[112:127]\n"
    "v_add_f32 v28, v28, v141\n"
    "v_cvt_pk_bf16_f32 v138, v140, v141\n"
    "v_add_f32 v26, v26, v142\n"
    "v_add_f32 v28, v28, v143\n"
    "v_cvt_pk_bf16_f32 v139, v142, v143\n"
    "v_xor_b32 v19, 0x8000, v19\n"
    "s_memrealtime s[74:75]\n"
    "La_%=:\n"
    "s_waitcnt vmcnt(8) lgkmcnt(0)\n"
    "s_sub_i32 s78, s34, s74\n"
    "s_cmp_gt_i32 s78, 0\n"
    "s_cbranch_scc1 Lpacea_%=\n"
    "Lpacear_%=:\n"
    "s_barrier\n"
    "v_mfma_f32_32x32x16_bf16 v[224:239], a[0:3], v[32:35], v[160:175]\n"
    "ds_read_b64_tr_b16 a[64:65], v16 offset:0\n"
    "s_bitcmp1_b32 s98, s69\n"
    "v_exp_f32 v144, v144\n"
    "v_xor_b32 v8, 0x8000, v8\n"
    "v_exp_f32 v145, v145\n"
    "v_mfma_f32_32x32x16_bf16 v[224:239], a[4:7], v[36:39], v[224:239]\n"
    "s_cbranch_scc0 LaltaV_%=\n"
    "buffer_load_dwordx4 v24, s[44:47], s82 offen offset:0 nt lds\n"
    "buffer_load_dwordx4 v24, s[44:47], s82 offen offset:1024 nt lds\n"
    "buffer_load_dwordx4 v24, s[44:47], s82 offen offset:2048 nt lds\n"
    "buffer_load_dwordx4 v24, s[44:47], s82 offen offset:3072 nt lds\n"
    "LaV_%=:\n"
    "v_mfma_f32_32x32x16_bf16 v[224:239], a[8:11], v[40:43], v[224:239]\n"
    "ds_read_b64_tr_b16 a[66:67], v16 offset:2048\n"
    "s_add_u32 s88, s88, 16384\n"
    "s_and_b32 s88, s88, 0x1c000\n"
    "v_exp_f32 v146, v146\n"
    "v_add_f32 v26, v26, v144\n"
    "v_mfma_f32_32x32x16_bf16 v[224:239], a[12:15], v[44:47], v[224:239]\n"
    "s_add_u32 s82, s89, s88\n"
    "s_add_u32 s93, s92, 65536\n"
    "v_exp_f32 v147, v147\n"
    "v_xor_b32 v9, 0x8000, v9\n"
    "v_add_f32 v28, v28, v145\n"
    "v_cvt_pk_bf16_f32 v144, v144, v145\n"
    "v_mfma_f32_32x32x16_bf16 v[224:239], a[16:19], v[48:51], v[224:239]\n"
    "ds_read_b64_tr_b16 a[68:69], v17 offset:0\n"
    "s_mov_b32 m0, s92\n"
    "s_bitcmp1_b32 s99, s69\n"
    "v_exp_f32 v148, v148\n"
    "v_add_f32 v26, v26, v146\n"
    "v_mfma_f32_32x32x16_bf16 v[224:239], a[20:23], v[52:55], v[224:239]\n"
    "s_cbranch_scc0 LaltaK_%=\n"
    "buffer_load_dwordx4 v20, s[40:43], s82 offen offset:0 nt lds\n"
    "buffer_load_dwordx4 v21, s[40:43], s82 offen offset:1024 nt lds\n"
    "buffer_load_dwordx4 v22, s[40:43], s82 offen offset:2048 nt lds\n"
    "buffer_load_dwordx4 v23, s[40:43], s82 offen offset:3072 nt lds\n"
    "LaK_%=:\n"
    "v_mfma_f32_32x32x16_bf16 v[224:239], a[24:27], v[56:59], v[224:239]\n"
    "ds_read_b64_tr_b16 a[70:71], v17 offset:2048\n"
    "v_exp_f32 v149, v149\n"
    "v_xor_b32 v10, 0x8000, v10\n"
    "v_add_f32 v28, v28, v147\n"
    "v_cvt_pk_bf16_f32 v145, v146, v147\n"
    "v_exp_f32 v150, v150\n"
    "v_mfma_f32_32x32x16_bf16 v[224:239], a[28:31], v[60:63], v[224:239]\n"
    "v_add_f32 v26, v26, v148\n"
    "v_exp_f32 v151, v151\n"
    "v_xor_b32 v11, 0x8000, v11\n"
    "v_add_f32 v28, v28, v149\n"
    "v_cvt_pk_bf16_f32 v146, v148, v149\n"
    "v_mfma_f32_32x32x16_bf16 v[240:255], a[32:35], v[32:35], v[160:175]\n"
    "ds_read_b64_tr_b16 a[72:73], v18 offset:0\n"
    "v_exp_f32 v152, v152\n"
    "v_add_f32 v26, v26, v150\n"
    "v_exp_f32 v153, v153\n"
    "v_mfma_f32_32x32x16_bf16 v[240:255], a[36:39], v[36:39], v[240:255]\n"
    "ds_read_b64_tr_b16 a[74:75], v18 offset:2048\n"
    "v_add_f32 v28, v28, v151\n"
    "v_xor_b32 v12, 0x8000, v12\n"
    "v_cvt_pk_bf16_f32 v147, v150, v151\n"
    "v_exp_f32 v154, v154\n"
    "v_mfma_f32_32x32x16_bf16 v[240:255], a[40:43], v[40:43], v[240:255]\n"
    "ds_read_b64_tr_b16 a[76:77], v19 offset:0\n"
    "s_add_u32 s34, s74, s84\n"
    "v_add_f32 v26, v26, v152\n"
    "v_exp_f32 v155, v155\n"
    "v_add_f32 v28, v28, v153\n"
    "v_mfma_f32_32x32x16_bf16 v[240:255], a[44:47], v[44:47], v[240:255]\n"
    "v_cvt_pk_bf16_f32 v152, v152, v153\n"
    "v_xor_b32 v13, 0x8000, v13\n"
    "v_exp_f32 v156, v156\n"
    "v_add_f32 v26, v26, v154\n"
    "v_mfma_f32_32x32x16_bf16 v[240:255], a[48:51], v[48:51], v[240:255]\n"
    "ds_read_b64_tr_b16 a[78:79], v19 offset:2048\n"
    "v_exp_f32 v157, v157\n"
    "v_add_f32 v28, v28, v155\n"
    "v_cvt_pk_bf16_f32 v153, v154, v155\n"
    "v_xor_b32 v14, 0x8000, v14\n"
    "v_mfma_f32_32x32x16_bf16 v[240:255], a[52:55], v[52:55], v[240:255]\n"
    "ds_read_b64_tr_b16 a[80:81], v16 offset:4096\n"
    "v_exp_f32 v158, v158\n"
    "v_add_f32 v26, v26, v156\n"
    "v_exp_f32 v159, v159\n"
    "v_add_f32 v28, v28, v157\n"
    "v_mfma_f32_32x32x16_bf16 v[240:255], a[56:59], v[56:59], v[240:255]\n"
    "v_cvt_pk_bf16_f32 v154, v156, v157\n"
    "v_add_f32 v26, v26, v158\n"
    "v_add_f32 v28, v28, v159\n"
    "v_cvt_pk_bf16_f32 v155, v158, v159\n"
    "v_add_f32 v26, v26, v28\n"
    "v_xor_b32 v15, 0x8000, v15\n"
    "v_mfma_f32_32x32x16_bf16 v[240:255], a[60:63], v[60:63], v[240:255]\n"
    "ds_read_b64_tr_b16 a[82:83], v16 offset:6144\n"
    "v_cmp_nge_f32 vcc, s76, v26\n"
    "s_nop 0\n"
    "s_cbranch_vccnz Lrxa_%=\n"
    "Lrxar_%=:\n"
    "v_add_f32 v25, v25, v26\n"
    "s_waitcnt lgkmcnt(2)\n"
    "v_mfma_f32_32x32x16_bf16 v[64:79], a[64:67], v[128:131], v[64:79]\n"
    "ds_read_b64_tr_b16 a[84:85], v17 offset:4096\n"
    "ds_read_b64_tr_b16 a[86:87], v17 offset:6144\n"
    "ds_read_b64_tr_b16 a[88:89], v18 offset:4096\n"
    "ds_read_b64_tr_b16 a[90:91], v18 offset:6144\n"
    "ds_read_b64_tr_b16 a[92:93], v19 offset:4096\n"
    "ds_read_b64_tr_b16 a[94:95], v19 offset:6144\n"
    "v_mfma_f32_32x32x16_bf16 v[80:95], a[68:71], v[128:131], v[80:95]\n"
    "ds_read_b64_tr_b16 a[96:97], v16 offset:8192\n"
    "ds_read_b64_tr_b16 a[98:99], v16 offset:10240\n"
    "v_exp_f32 v224, v224\n"
    "v_exp_f32 v225, v225\n"
    "v_mfma_f32_32x32x16_bf16 v[96:111], a[72:75], v[128:131], v[96:111]\n"
    "ds_read_b128 a[0:3], v8 offset:0\n"
    "ds_read_b64_tr_b16 a[100:101], v17 offset:8192\n"
    "ds_read_b64_tr_b16 a[102:103], v17 offset:10240\n"
    "v_exp_f32 v226, v226\n"
    "v_exp_f32 v227, v227\n"
    "v_mfma_f32_32x32x16_bf16 v[112:127], a[76:79], v[128:131], v[112:127]\n"
    "ds_read_b64_tr_b16 a[104:105], v18 offset:8192\n"
    "ds_read_b128 a[4:7], v9 offset:0\n"
    "ds_read_b64_tr_b16 a[106:107], v18 offset:10240\n"
    "ds_read_b64_tr_b16 a[108:109], v19 offset:8192\n"
    "v_exp_f32 v228, v228\n"
    "s_waitcnt lgkmcnt(9)\n"
    "v_mfma_f32_32x32x16_bf16 v[64:79], a[80:83], v[136:139], v[64:79]\n"
    "ds_read_b128 a[8:11], v10 offset:0\n"
    "ds_read_b64_tr_b16 a[110:111], v19 offset:10240\n"
    "ds_read_b64_tr_b16 a[112:113], v16 offset:12288\n"
    "v_add_f32 v26, v224, v226\n"
    "v_exp_f32 v229, v229\n"
    "v_mfma_f32_32x32x16_bf16 v[80:95], a[84:87], v[136:139], v[80:95]\n"
    "ds_read_b64_tr_b16 a[114:115], v16 offset:14336\n"
    "ds_read_b64_tr_b16 a[116:117], v17 offset:12288\n"
    "ds_read_b64_tr_b16 a[118:119], v17 offset:14336\n"
    "ds_read_b64_tr_b16 a[120:121], v18 offset:12288\n"
    "ds_read_b64_tr_b16 a[122:123], v18 offset:14336\n"
    "ds_read_b64_tr_b16 a[124:125], v19 offset:12288\n"
    "ds_read_b64_tr_b16 a[126:127], v19 offset:14336\n"
    "v_mfma_f32_32x32x16_bf16 v[96:111], a[88:91], v[136:139], v[96:111]\n"
    "ds_read_b128 a[12:15], v11 offset:0\n"
    "ds_read_b128 a[16:19], v12 offset:0\n"
    "v_add_f32 v28, v225, v227\n"
    "v_cvt_pk_bf16_f32 v224, v224, v225\n"
    "v_cvt_pk_bf16_f32 v225, v226, v227\n"
    "v_mfma_f32_32x32x16_bf16 v[112:127], a[92:95], v[136:139], v[112:127]\n"
    "ds_read_b128 a[20:23], v13 offset:0\n"
    "v_exp_f32 v230, v230\n"
    "v_add_f32 v26, v26, v228\n"
    "v_exp_f32 v231, v231\n"
    "s_waitcnt lgkmcnt(11)\n"
    "v_mfma_f32_32x32x16_bf16 v[64:79], a[96:99], v[144:147], v[64:79]\n"
    "ds_read_b128 a[24:27], v14 offset:0\n"
    "ds_read_b128 a[28:31], v15 offset:0\n"
    "v_add_f32 v28, v28, v229\n"
    "v_cvt_pk_bf16_f32 v226, v228, v229\n"
    "v_exp_f32 v232, v232\n"
    "v_mfma_f32_32x32x16_bf16 v[80:95], a[100:103], v[144:147], v[80:95]\n"
    "ds_read_b128 a[32:35], v8 offset:8192\n"
    "ds_read_b128 a[36:39], v9 offset:8192\n"
    "v_add_f32 v26, v26, v230\n"
    "v_exp_f32 v233, v233\n"
    "v_add_f32 v28, v28, v231\n"
    "v_mfma_f32_32x32x16_bf16 v[96:111], a[104:107], v[144:147], v[96:111]\n"
    "ds_read_b128 a[40:43], v10 offset:8192\n"
    "s_add_u32 s92, s92, 16384\n"
    "v_cvt_pk_bf16_f32 v227, v230, v231\n"
    "v_exp_f32 v234, v234\n"
    "v_add_f32 v26, v26, v232\n"
    "v_mfma_f32_32x32x16_bf16 v[112:127], a[108:111], v[144:147], v[112:127]\n"
    "ds_read_b128 a[44:47], v11 offset:8192\n"
    "ds_read_b128 a[48:51], v12 offset:8192\n"
    "s_and_b32 s92, s92, 0xffff\n"
    "v_exp_f32 v235, v235\n"
    "v_add_f32 v28, v28, v233\n"
    "s_waitcnt lgkmcnt(10)\n"
    "v_mfma_f32_32x32x16_bf16 v[64:79], a[112:115], v[152:155], v[64:79]\n"
    "ds_read_b128 a[52:55], v13 offset:8192\n"
    "s_mov_b32 m0, s93\n"
    "v_cvt_pk_bf16_f32 v232, v232, v233\n"
    "v_exp_f32 v236, v236\n"
    "v_add_f32 v26, v26, v234\n"
    "v_mfma_f32_32x32x16_bf16 v[80:95], a[116:119], v[152:155], v[80:95]\n"
    "ds_read_b128 a[56:59], v14 offset:8192\n"
    "ds_read_b128 a[60:63], v15 offset:8192\n"
    "s_add_u32 s69, s69, 1\n"
    "v_exp_f32 v237, v237\n"
    "v_add_f32 v28, v28, v235\n"
    "v_mfma_f32_32x32x16_bf16 v[96:111], a[120:123], v[152:155], v[96:111]\n"
    "s_bitcmp1_b32 0x8080, s69\n"
    "v_cvt_pk_bf16_f32 v233, v234, v235\n"
    "v_exp_f32 v238, v238\n"
    "v_add_f32 v26, v26, v236\n"
    "v_mfma_f32_32x32x16_bf16 v[112:127], a[124:127], v[152:155], v[112:127]\n"
    "v_exp_f32 v239, v239\n"
    "v_add_f32 v28, v28, v237\n"
    "v_cvt_pk_bf16_f32 v234, v236, v237\n"
    "v_add_f32 v26, v26, v238\n"
    "v_add_f32 v28, v28, v239\n"
    "v_cvt_pk_bf16_f32 v235, v238, v239\n"
    "s_memrealtime s[74:75]\n"
    "s_cbranch_scc0 Lb_%=\n"
    "Llast_%=:\n"
    "v_exp_f32 v240, v240\n"
    "v_exp_f32 v241, v241\n"
    "v_exp_f32 v242, v242\n"
    "v_add_f32 v26, v26, v240\n"
    "v_exp_f32 v243, v243\n"
    "v_add_f32 v28, v28, v241\n"
    "v_cvt_pk_bf16_f32 v240, v240, v241\n"
    "v_exp_f32 v244, v244\n"
    "v_add_f32 v26, v26, v242\n"
    "v_exp_f32 v245, v245\n"
    "v_add_f32 v28, v28, v243\n"
    "v_cvt_pk_bf16_f32 v241, v242, v243\n"
    "v_exp_f32 v246, v246\n"
    "v_add_f32 v26, v26, v244\n"
    "v_exp_f32 v247, v247\n"
    "v_add_f32 v28, v28, v245\n"
    "v_cvt_pk_bf16_f32 v242, v244, v245\n"
    "v_exp_f32 v248, v248\n"
    "v_add_f32 v26, v26, v246\n"
    "v_exp_f32 v249, v249\n"
    "v_add_f32 v28, v28, v247\n"
    "v_cvt_pk_bf16_f32 v243, v246, v247\n"
    "v_exp_f32 v250, v250\n"
    "v_add_f32 v26, v26, v248\n"
    "v_exp_f32 v251, v251\n"
    "v_add_f32 v28, v28, v249\n"
    "v_cvt_pk_bf16_f32 v248, v248, v249\n"
    "v_exp_f32 v252, v252\n"
    "v_add_f32 v26, v26, v250\n"
    "v_exp_f32 v253, v253\n"
    "v_add_f32 v28, v28, v251\n"
    "v_cvt_pk_bf16_f32 v249, v250, v251\n"
    "v_exp_f32 v254, v254\n"
    "v_add_f32 v26, v26, v252\n"
    "v_exp_f32 v255, v255\n"
    "v_add_f32 v28, v28, v253\n"
    "v_cvt_pk_bf16_f32 v250, v252, v253\n"
    "v_add_f32 v26, v26, v254\n"
    "v_add_f32 v28, v28, v255\n"
    "v_cvt_pk_bf16_f32 v251, v254, v255\n"
    "v_add_f32 v26, v26, v28\n"
    "v_cmp_nge_f32 vcc, s76, v26\n"
    "s_nop 0\n"
    "s_cbranch_vccz Lrxlr_%=\n"
    "s_call_b64 s[80:81], Lrxl_%=\n"
    "Lrxlr_%=:\n"
    "v_add_f32 v25, v25, v26\n"
    "v_mov_b32 v27, v25\n"
    "s_nop 1\n"
    "v_permlane32_swap_b32 v25, v27\n"
    "v_add_f32 v25, v25, v27\n"
    "v_rcp_f32 v30, v25\n"
    "v_lshrrev_b32 v1, 5, v0\n"
    "v_and_b32 v4, 15, v0\n"
    "v_xor_b32 v1, v1, v4\n"
    "v_lshlrev_b32 v1, 4, v1\n"
    "v_and_b32 v2, 31, v0\n"
    "v_lshl_add_u32 v1, v2, 10, v1\n"
    "s_lshl_b32 s78, s68, 8\n"
    "s_cmp_eq_u32 s87, 0\n"
    "s_cselect_b32 s79, 131072, 0\n"
    "s_add_u32 s78, s78, s79\n"
    "v_add_u32 v32, s78, v1\n"
    "s_cmp_eq_u32 s87, 0\n"
    "s_cbranch_scc1 Ll0_%=\n"
    "v_lshrrev_b32 v5, 5, v0\n"
    "v_lshlrev_b32 v48, 14, v5\n"
    "v_bfe_u32 v5, v0, 4, 1\n"
    "v_lshl_add_u32 v48, v5, 12, v48\n"
    "v_and_b32 v4, 15, v0\n"
    "v_lshl_add_u32 v48, v4, 4, v48\n"
    "v_add_u32 v48, s94, v48\n"
    "v_add_u32 v49, 1024, v48\n"
    "v_add_u32 v50, 2048, v48\n"
    "v_add_u32 v51, 3072, v48\n"
    "v_add_u32 v52, 8192, v48\n"
    "v_add_u32 v53, 9216, v48\n"
    "v_add_u32 v54, 10240, v48\n"
    "v_add_u32 v55, 11264, v48\n"
    "v_mov_b32 v62, 0x05040100\n"
    "v_mov_b32 v63, 0x07060302\n"
    "v_bfe_u32 v5, v0, 4, 1\n"
    "v_lshlrev_b32 v4, 10, v5\n"
    "v_lshlrev_b32 v5, 4, v5\n"
    "v_xor_b32 v48, v4, v48\n"
    "v_xor_b32 v49, v4, v49\n"
    "v_xor_b32 v50, v4, v50\n"
    "v_xor_b32 v51, v4, v51\n"
    "v_xor_b32 v52, v4, v52\n"
    "v_xor_b32 v53, v4, v53\n"
    "v_xor_b32 v54, v4, v54\n"
    "v_xor_b32 v55, v4, v55\n"
    "v_alignbit_b32 v62, v62, v62, v5\n"
    "v_alignbit_b32 v63, v63, v63, v5\n"
    "s_mov_b64 s[48:49], s[52:53]\n"
    "s_lshl_b32 s85, s65, 15\n"
    "s_mov_b32 s90, 32768\n"
    "s_mov_b32 s91, 0x7ffff\n"
    "v_lshrrev_b32 v5, 5, v0\n"
    "v_lshlrev_b32 v1, 14, v5\n"
    "v_bfe_u32 v5, v0, 4, 1\n"
    "v_lshl_add_u32 v1, v5, 12, v1\n"
    "v_and_b32 v4, 15, v0\n"
    "v_lshl_add_u32 v1, v4, 4, v1\n"
    "v_add_u32 v1, s94, v1\n"
    "s_add_u32 s78, s65, 2\n"
    "s_and_b32 s78, s78, 15\n"
    "s_lshl_b32 s78, s78, 15\n"
    "s_add_u32 s78, s78, 8192\n"
    "v_bfe_u32 v2, v0, 4, 1\n"
    "v_lshlrev_b32 v2, 10, v2\n"
    "v_sub_u32 v4, v1, v2\n"
    "v_add_u32 v1, v1, v2\n"
    "buffer_load_dwordx4 v[208:211], v1, s[52:55], s78 offen offset:0\n"
    "buffer_load_dwordx4 v[212:215], v4, s[52:55], s78 offen offset:1024\n"
    "buffer_load_dwordx4 v[216:219], v1, s[52:55], s78 offen offset:2048\n"
    "buffer_load_dwordx4 v[220:223], v4, s[52:55], s78 offen offset:3072\n"
    "s_waitcnt vmcnt(10)\n"
    "s_branch Ll1_%=\n"
    "Ll0_%=:\n"
    "s_waitcnt vmcnt(8)\n"
    "Ll1_%=:\n"
    "s_waitcnt lgkmcnt(0)\n"
    "s_barrier\n"
    "s_cmp_eq_u32 s87, 0\n"
    "ds_read_b64_tr_b16 a[64:65], v16 offset:16384\n"
    "ds_read_b64_tr_b16 a[66:67], v16 offset:18432\n"
    "ds_read_b64_tr_b16 a[80:81], v16 offset:20480\n"
    "ds_read_b64_tr_b16 a[82:83], v16 offset:22528\n"
    "ds_read_b64_tr_b16 a[96:97], v16 offset:24576\n"
    "ds_read_b64_tr_b16 a[98:99], v16 offset:26624\n"
    "ds_read_b64_tr_b16 a[112:113], v16 offset:28672\n"
    "ds_read_b64_tr_b16 a[114:115], v16 offset:30720\n"
    "ds_read_b64_tr_b16 a[68:69], v17 offset:16384\n"
    "ds_read_b64_tr_b16 a[70:71], v17 offset:18432\n"
    "ds_read_b64_tr_b16 a[84:85], v17 offset:20480\n"
    "ds_read_b64_tr_b16 a[86:87], v17 offset:22528\n"
    "s_waitcnt lgkmcnt(4)\n"
    "v_mfma_f32_32x32x16_bf16 v[64:79], a[64:67], v[224:227], v[64:79]\n"
    "s_cbranch_scc1 Llw0_%=\n"
    "buffer_load_dwordx4 v[128:131], v48, s[48:51], s85 offen\n"
    "Llw0_%=:\n"
    "ds_read_b64_tr_b16 a[100:101], v17 offset:24576\n"
    "ds_read_b64_tr_b16 a[102:103], v17 offset:26624\n"
    "v_xor_b32 v33, 32, v32\n"
    "v_xor_b32 v34, 64, v32\n"
    "v_mfma_f32_32x32x16_bf16 v[64:79], a[80:83], v[232:235], v[64:79]\n"
    "ds_read_b64_tr_b16 a[116:117], v17 offset:28672\n"
    "ds_read_b64_tr_b16 a[118:119], v17 offset:30720\n"
    "v_xor_b32 v35, 96, v32\n"
    "v_mfma_f32_32x32x16_bf16 v[64:79], a[96:99], v[240:243], v[64:79]\n"
    "s_cbranch_scc1 Llw1_%=\n"
    "buffer_load_dwordx4 v[132:135], v49, s[48:51], s85 offen\n"
    "Llw1_%=:\n"
    "ds_read_b64_tr_b16 a[72:73], v18 offset:16384\n"
    "ds_read_b64_tr_b16 a[74:75], v18 offset:18432\n"
    "v_xor_b32 v36, 128, v32\n"
    "v_mfma_f32_32x32x16_bf16 v[64:79], a[112:115], v[248:251], v[64:79]\n"
    "ds_read_b64_tr_b16 a[88:89], v18 offset:20480\n"
    "ds_read_b64_tr_b16 a[90:91], v18 offset:22528\n"
    "v_xor_b32 v37, 160, v32\n"
    "s_waitcnt lgkmcnt(4)\n"
    "v_mfma_f32_32x32x16_bf16 v[80:95], a[68:71], v[224:227], v[80:95]\n"
    "s_cbranch_scc1 Llw2_%=\n"
    "buffer_load_dwordx4 v[136:139], v50, s[48:51], s85 offen\n"
    "Llw2_%=:\n"
    "ds_read_b64_tr_b16 a[104:105], v18 offset:24576\n"
    "ds_read_b64_tr_b16 a[106:107], v18 offset:26624\n"
    "v_xor_b32 v38, 192, v32\n"
    "v_mfma_f32_32x32x16_bf16 v[80:95], a[84:87], v[232:235], v[80:95]\n"
    "ds_read_b64_tr_b16 a[120:121], v18 offset:28672\n"
    "ds_read_b64_tr_b16 a[122:123], v18 offset:30720\n"
    "v_xor_b32 v39, 224, v32\n"
    "v_mfma_f32_32x32x16_bf16 v[80:95], a[100:103], v[240:243], v[80:95]\n"
    "s_cbranch_scc1 Llw3_%=\n"
    "buffer_load_dwordx4 v[140:143], v51, s[48:51], s85 offen\n"
    "Llw3_%=:\n"
    "ds_read_b64_tr_b16 a[76:77], v19 offset:16384\n"
    "ds_read_b64_tr_b16 a[78:79], v19 offset:18432\n"
    "v_mfma_f32_32x32x16_bf16 v[80:95], a[116:119], v[248:251], v[80:95]\n"
    "ds_read_b64_tr_b16 a[92:93], v19 offset:20480\n"
    "ds_read_b64_tr_b16 a[94:95], v19 offset:22528\n"
    "v_pk_mul_f32 v[64:65], v[64:65], v[30:31] op_sel_hi:[1,0]\n"
    "v_pk_mul_f32 v[66:67], v[66:67], v[30:31] op_sel_hi:[1,0]\n"
    "v_pk_mul_f32 v[68:69], v[68:69], v[30:31] op_sel_hi:[1,0]\n"
    "v_pk_mul_f32 v[70:71], v[70:71], v[30:31] op_sel_hi:[1,0]\n"
    "v_cvt_pk_bf16_f32 v64, v64, v65\n"
    "s_waitcnt lgkmcnt(4)\n"
    "v_mfma_f32_32x32x16_bf16 v[96:111], a[72:75], v[224:227], v[96:111]\n"
    "s_cbranch_scc1 Llw4_%=\n"
    "buffer_load_dwordx4 v[144:147], v52, s[48:51], s85 offen\n"
    "Llw4_%=:\n"
    "ds_read_b64_tr_b16 a[108:109], v19 offset:24576\n"
    "ds_read_b64_tr_b16 a[110:111], v19 offset:26624\n"
    "v_cvt_pk_bf16_f32 v65, v66, v67\n"
    "v_cvt_pk_bf16_f32 v66, v68, v69\n"
    "v_cvt_pk_bf16_f32 v67, v70, v71\n"
    "ds_write_b128 v32, v[64:67]\n"
    "v_mfma_f32_32x32x16_bf16 v[96:111], a[88:91], v[232:235], v[96:111]\n"
    "ds_read_b64_tr_b16 a[124:125], v19 offset:28672\n"
    "ds_read_b64_tr_b16 a[126:127], v19 offset:30720\n"
    "v_pk_mul_f32 v[72:73], v[72:73], v[30:31] op_sel_hi:[1,0]\n"
    "v_pk_mul_f32 v[74:75], v[74:75], v[30:31] op_sel_hi:[1,0]\n"
    "v_pk_mul_f32 v[76:77], v[76:77], v[30:31] op_sel_hi:[1,0]\n"
    "v_pk_mul_f32 v[78:79], v[78:79], v[30:31] op_sel_hi:[1,0]\n"
    "v_cvt_pk_bf16_f32 v72, v72, v73\n"
    "v_mfma_f32_32x32x16_bf16 v[96:111], a[104:107], v[240:243], v[96:111]\n"
    "s_cbranch_scc1 Llw5_%=\n"
    "buffer_load_dwordx4 v[148:151], v53, s[48:51], s85 offen\n"
    "Llw5_%=:\n"
    "v_cvt_pk_bf16_f32 v73, v74, v75\n"
    "v_cvt_pk_bf16_f32 v74, v76, v77\n"
    "v_cvt_pk_bf16_f32 v75, v78, v79\n"
    "ds_write_b128 v33, v[72:75]\n"
    "v_mfma_f32_32x32x16_bf16 v[96:111], a[120:123], v[248:251], v[96:111]\n"
    "v_pk_mul_f32 v[80:81], v[80:81], v[30:31] op_sel_hi:[1,0]\n"
    "v_pk_mul_f32 v[82:83], v[82:83], v[30:31] op_sel_hi:[1,0]\n"
    "v_pk_mul_f32 v[84:85], v[84:85], v[30:31] op_sel_hi:[1,0]\n"
    "v_pk_mul_f32 v[86:87], v[86:87], v[30:31] op_sel_hi:[1,0]\n"
    "v_cvt_pk_bf16_f32 v80, v80, v81\n"
    "s_waitcnt lgkmcnt(1)\n"
    "v_mfma_f32_32x32x16_bf16 v[112:127], a[76:79], v[224:227], v[112:127]\n"
    "s_cbranch_scc1 Llw6_%=\n"
    "buffer_load_dwordx4 v[152:155], v54, s[48:51], s85 offen\n"
    "Llw6_%=:\n"
    "v_cvt_pk_bf16_f32 v81, v82, v83\n"
    "v_cvt_pk_bf16_f32 v82, v84, v85\n"
    "v_cvt_pk_bf16_f32 v83, v86, v87\n"
    "ds_write_b128 v34, v[80:83]\n"
    "v_mfma_f32_32x32x16_bf16 v[112:127], a[92:95], v[232:235], v[112:127]\n"
    "v_pk_mul_f32 v[88:89], v[88:89], v[30:31] op_sel_hi:[1,0]\n"
    "v_pk_mul_f32 v[90:91], v[90:91], v[30:31] op_sel_hi:[1,0]\n"
    "v_pk_mul_f32 v[92:93], v[92:93], v[30:31] op_sel_hi:[1,0]\n"
    "v_pk_mul_f32 v[94:95], v[94:95], v[30:31] op_sel_hi:[1,0]\n"
    "v_cvt_pk_bf16_f32 v88, v88, v89\n"
    "v_mfma_f32_32x32x16_bf16 v[112:127], a[108:111], v[240:243], v[112:127]\n"
    "s_cbranch_scc1 Llw7_%=\n"
    "buffer_load_dwordx4 v[156:159], v55, s[48:51], s85 offen\n"
    "Llw7_%=:\n"
    "v_cvt_pk_bf16_f32 v89, v90, v91\n"
    "v_cvt_pk_bf16_f32 v90, v92, v93\n"
    "v_cvt_pk_bf16_f32 v91, v94, v95\n"
    "ds_write_b128 v35, v[88:91]\n"
    "v_mfma_f32_32x32x16_bf16 v[112:127], a[124:127], v[248:251], v[112:127]\n"
    "v_xor_b32 v16, 0x8000, v16\n"
    "v_xor_b32 v17, 0x8000, v17\n"
    "v_xor_b32 v18, 0x8000, v18\n"
    "v_xor_b32 v19, 0x8000, v19\n"
    "v_pk_mul_f32 v[96:97], v[96:97], v[30:31] op_sel_hi:[1,0]\n"
    "v_pk_mul_f32 v[98:99], v[98:99], v[30:31] op_sel_hi:[1,0]\n"
    "v_pk_mul_f32 v[100:101], v[100:101], v[30:31] op_sel_hi:[1,0]\n"
    "v_pk_mul_f32 v[102:103], v[102:103], v[30:31] op_sel_hi:[1,0]\n"
    "v_cvt_pk_bf16_f32 v96, v96, v97\n"
    "v_cvt_pk_bf16_f32 v97, v98, v99\n"
    "v_cvt_pk_bf16_f32 v98, v100, v101\n"
    "v_cvt_pk_bf16_f32 v99, v102, v103\n"
    "ds_write_b128 v36, v[96:99]\n"
    "v_pk_mul_f32 v[104:105], v[104:105], v[30:31] op_sel_hi:[1,0]\n"
    "v_pk_mul_f32 v[106:107], v[106:107], v[30:31] op_sel_hi:[1,0]\n"
    "v_pk_mul_f32 v[108:109], v[108:109], v[30:31] op_sel_hi:[1,0]\n"
    "v_pk_mul_f32 v[110:111], v[110:111], v[30:31] op_sel_hi:[1,0]\n"
    "v_cvt_pk_bf16_f32 v104, v104, v105\n"
    "v_cvt_pk_bf16_f32 v105, v106, v107\n"
    "v_cvt_pk_bf16_f32 v106, v108, v109\n"
    "v_cvt_pk_bf16_f32 v107, v110, v111\n"
    "ds_write_b128 v37, v[104:107]\n"
    "v_pk_mul_f32 v[112:113], v[112:113], v[30:31] op_sel_hi:[1,0]\n"
    "v_pk_mul_f32 v[114:115], v[114:115], v[30:31] op_sel_hi:[1,0]\n"
    "v_pk_mul_f32 v[116:117], v[116:117], v[30:31] op_sel_hi:[1,0]\n"
    "v_pk_mul_f32 v[118:119], v[118:119], v[30:31] op_sel_hi:[1,0]\n"
    "v_cvt_pk_bf16_f32 v112, v112, v113\n"
    "v_cvt_pk_bf16_f32 v113, v114, v115\n"
    "v_cvt_pk_bf16_f32 v114, v116, v117\n"
    "v_cvt_pk_bf16_f32 v115, v118, v119\n"
    "ds_write_b128 v38, v[112:115]\n"
    "v_pk_mul_f32 v[120:121], v[120:121], v[30:31] op_sel_hi:[1,0]\n"
    "v_pk_mul_f32 v[122:123], v[122:123], v[30:31] op_sel_hi:[1,0]\n"
    "v_pk_mul_f32 v[124:125], v[124:125], v[30:31] op_sel_hi:[1,0]\n"
    "v_pk_mul_f32 v[126:127], v[126:127], v[30:31] op_sel_hi:[1,0]\n"
    "v_cvt_pk_bf16_f32 v120, v120, v121\n"
    "v_cvt_pk_bf16_f32 v121, v122, v123\n"
    "v_cvt_pk_bf16_f32 v122, v124, v125\n"
    "v_cvt_pk_bf16_f32 v123, v126, v127\n"
    "ds_write_b128 v39, v[120:123]\n"
    "s_add_u32 s69, s69, 1\n"
    "s_cmp_eq_u32 s87, 0\n"
    "s_cbranch_scc0 Lostart_%=\n"
    "s_mov_b32 m0, s93\n"
    "s_nop 0\n"
    "buffer_load_dwordx4 v24, s[44:47], s82 offen offset:0 nt lds\n"
    "buffer_load_dwordx4 v24, s[44:47], s82 offen offset:1024 nt lds\n"
    "buffer_load_dwordx4 v24, s[44:47], s82 offen offset:2048 nt lds\n"
    "buffer_load_dwordx4 v24, s[44:47], s82 offen offset:3072 nt lds\n"
    "ds_read_b128 a[0:3], v8 offset:0\n"
    "ds_read_b128 a[4:7], v9 offset:0\n"
    "ds_read_b128 a[8:11], v10 offset:0\n"
    "ds_read_b128 a[12:15], v11 offset:0\n"
    "ds_read_b128 a[16:19], v12 offset:0\n"
    "ds_read_b128 a[20:23], v13 offset:0\n"
    "ds_read_b128 a[24:27], v14 offset:0\n"
    "ds_read_b128 a[28:31], v15 offset:0\n"
    "ds_read_b128 a[32:35], v8 offset:8192\n"
    "ds_read_b128 a[36:39], v9 offset:8192\n"
    "ds_read_b128 a[40:43], v10 offset:8192\n"
    "ds_read_b128 a[44:47], v11 offset:8192\n"
    "ds_read_b128 a[48:51], v12 offset:8192\n"
    "ds_read_b128 a[52:55], v13 offset:8192\n"
    "ds_read_b128 a[56:59], v14 offset:8192\n"
    "ds_read_b128 a[60:63], v15 offset:8192\n"
    "s_waitcnt lgkmcnt(0)\n"
    "s_mov_b32 s74, 0x3e0293ee\n"
    "s_mov_b32 s75, 0x3e0293ee\n"
    "v_accvgpr_read_b32 v224, a128\n"
    "v_accvgpr_read_b32 v225, a129\n"
    "v_accvgpr_read_b32 v226, a132\n"
    "v_accvgpr_read_b32 v227, a133\n"
    "v_accvgpr_read_b32 v228, a136\n"
    "v_accvgpr_read_b32 v229, a137\n"
    "v_accvgpr_read_b32 v230, a140\n"
    "v_accvgpr_read_b32 v231, a141\n"
    "v_accvgpr_read_b32 v232, a144\n"
    "v_accvgpr_read_b32 v233, a145\n"
    "v_accvgpr_read_b32 v234, a148\n"
    "v_accvgpr_read_b32 v235, a149\n"
    "v_accvgpr_read_b32 v236, a152\n"
    "v_accvgpr_read_b32 v237, a153\n"
    "v_accvgpr_read_b32 v238, a156\n"
    "v_accvgpr_read_b32 v239, a157\n"
    "v_accvgpr_read_b32 v240, a160\n"
    "v_accvgpr_read_b32 v241, a161\n"
    "v_accvgpr_read_b32 v242, a164\n"
    "v_accvgpr_read_b32 v243, a165\n"
    "v_accvgpr_read_b32 v244, a168\n"
    "v_accvgpr_read_b32 v245, a169\n"
    "v_accvgpr_read_b32 v246, a172\n"
    "v_accvgpr_read_b32 v247, a173\n"
    "v_accvgpr_read_b32 v248, a176\n"
    "v_accvgpr_read_b32 v249, a177\n"
    "v_accvgpr_read_b32 v250, a180\n"
    "v_accvgpr_read_b32 v251, a181\n"
    "v_accvgpr_read_b32 v252, a184\n"
    "v_accvgpr_read_b32 v253, a185\n"
    "v_accvgpr_read_b32 v254, a188\n"
    "v_accvgpr_read_b32 v255, a189\n"
    "v_pk_mul_f32 v[224:225], v[224:225], s[74:75]\n"
    "v_pk_mul_f32 v[226:227], v[226:227], s[74:75]\n"
    "v_pk_mul_f32 v[228:229], v[228:229], s[74:75]\n"
    "v_pk_mul_f32 v[230:231], v[230:231], s[74:75]\n"
    "v_pk_mul_f32 v[232:233], v[232:233], s[74:75]\n"
    "v_pk_mul_f32 v[234:235], v[234:235], s[74:75]\n"
    "v_pk_mul_f32 v[236:237], v[236:237], s[74:75]\n"
    "v_pk_mul_f32 v[238:239], v[238:239], s[74:75]\n"
    "v_pk_mul_f32 v[240:241], v[240:241], s[74:75]\n"
    "v_pk_mul_f32 v[242:243], v[242:243], s[74:75]\n"
    "v_pk_mul_f32 v[244:245], v[244:245], s[74:75]\n"
    "v_pk_mul_f32 v[246:247], v[246:247], s[74:75]\n"
    "v_pk_mul_f32 v[248:249], v[248:249], s[74:75]\n"
    "v_pk_mul_f32 v[250:251], v[250:251], s[74:75]\n"
    "v_pk_mul_f32 v[252:253], v[252:253], s[74:75]\n"
    "v_pk_mul_f32 v[254:255], v[254:255], s[74:75]\n"
    "v_cvt_pk_bf16_f32 v32, v224, v228\n"
    "v_cvt_pk_bf16_f32 v48, v226, v230\n"
    "v_cvt_pk_bf16_f32 v33, v232, v236\n"
    "v_cvt_pk_bf16_f32 v49, v234, v238\n"
    "v_cvt_pk_bf16_f32 v34, v240, v244\n"
    "v_cvt_pk_bf16_f32 v50, v242, v246\n"
    "v_cvt_pk_bf16_f32 v35, v248, v252\n"
    "v_cvt_pk_bf16_f32 v51, v250, v254\n"
    "v_cvt_pk_bf16_f32 v36, v225, v229\n"
    "v_cvt_pk_bf16_f32 v52, v227, v231\n"
    "v_cvt_pk_bf16_f32 v37, v233, v237\n"
    "v_cvt_pk_bf16_f32 v53, v235, v239\n"
    "v_cvt_pk_bf16_f32 v38, v241, v245\n"
    "v_cvt_pk_bf16_f32 v54, v243, v247\n"
    "v_cvt_pk_bf16_f32 v39, v249, v253\n"
    "v_cvt_pk_bf16_f32 v55, v251, v255\n"
    "v_accvgpr_read_b32 v224, a130\n"
    "v_accvgpr_read_b32 v225, a131\n"
    "v_accvgpr_read_b32 v226, a134\n"
    "v_accvgpr_read_b32 v227, a135\n"
    "v_accvgpr_read_b32 v228, a138\n"
    "v_accvgpr_read_b32 v229, a139\n"
    "v_accvgpr_read_b32 v230, a142\n"
    "v_accvgpr_read_b32 v231, a143\n"
    "v_accvgpr_read_b32 v232, a146\n"
    "v_accvgpr_read_b32 v233, a147\n"
    "v_accvgpr_read_b32 v234, a150\n"
    "v_accvgpr_read_b32 v235, a151\n"
    "v_accvgpr_read_b32 v236, a154\n"
    "v_accvgpr_read_b32 v237, a155\n"
    "v_accvgpr_read_b32 v238, a158\n"
    "v_accvgpr_read_b32 v239, a159\n"
    "v_accvgpr_read_b32 v240, a162\n"
    "v_accvgpr_read_b32 v241, a163\n"
    "v_accvgpr_read_b32 v242, a166\n"
    "v_accvgpr_read_b32 v243, a167\n"
    "v_accvgpr_read_b32 v244, a170\n"
    "v_accvgpr_read_b32 v245, a171\n"
    "v_accvgpr_read_b32 v246, a174\n"
    "v_accvgpr_read_b32 v247, a175\n"
    "v_accvgpr_read_b32 v248, a178\n"
    "v_accvgpr_read_b32 v249, a179\n"
    "v_accvgpr_read_b32 v250, a182\n"
    "v_accvgpr_read_b32 v251, a183\n"
    "v_accvgpr_read_b32 v252, a186\n"
    "v_accvgpr_read_b32 v253, a187\n"
    "v_accvgpr_read_b32 v254, a190\n"
    "v_accvgpr_read_b32 v255, a191\n"
    "v_pk_mul_f32 v[224:225], v[224:225], s[74:75]\n"
    "v_pk_mul_f32 v[226:227], v[226:227], s[74:75]\n"
    "v_pk_mul_f32 v[228:229], v[228:229], s[74:75]\n"
    "v_pk_mul_f32 v[230:231], v[230:231], s[74:75]\n"
    "v_pk_mul_f32 v[232:233], v[232:233], s[74:75]\n"
    "v_pk_mul_f32 v[234:235], v[234:235], s[74:75]\n"
    "v_pk_mul_f32 v[236:237], v[236:237], s[74:75]\n"
    "v_pk_mul_f32 v[238:239], v[238:239], s[74:75]\n"
    "v_pk_mul_f32 v[240:241], v[240:241], s[74:75]\n"
    "v_pk_mul_f32 v[242:243], v[242:243], s[74:75]\n"
    "v_pk_mul_f32 v[244:245], v[244:245], s[74:75]\n"
    "v_pk_mul_f32 v[246:247], v[246:247], s[74:75]\n"
    "v_pk_mul_f32 v[248:249], v[248:249], s[74:75]\n"
    "v_pk_mul_f32 v[250:251], v[250:251], s[74:75]\n"
    "v_pk_mul_f32 v[252:253], v[252:253], s[74:75]\n"
    "v_pk_mul_f32 v[254:255], v[254:255], s[74:75]\n"
    "v_cvt_pk_bf16_f32 v40, v224, v228\n"
    "v_cvt_pk_bf16_f32 v56, v226, v230\n"
    "v_cvt_pk_bf16_f32 v41, v232, v236\n"
    "v_cvt_pk_bf16_f32 v57, v234, v238\n"
    "v_cvt_pk_bf16_f32 v42, v240, v244\n"
    "v_cvt_pk_bf16_f32 v58, v242, v246\n"
    "v_cvt_pk_bf16_f32 v43, v248, v252\n"
    "v_cvt_pk_bf16_f32 v59, v250, v254\n"
    "v_cvt_pk_bf16_f32 v44, v225, v229\n"
    "v_cvt_pk_bf16_f32 v60, v227, v231\n"
    "v_cvt_pk_bf16_f32 v45, v233, v237\n"
    "v_cvt_pk_bf16_f32 v61, v235, v239\n"
    "v_cvt_pk_bf16_f32 v46, v241, v245\n"
    "v_cvt_pk_bf16_f32 v62, v243, v247\n"
    "v_cvt_pk_bf16_f32 v47, v249, v253\n"
    "v_cvt_pk_bf16_f32 v63, v251, v255\n"
    "v_permlane16_swap_b32 v32, v48\n"
    "v_permlane16_swap_b32 v33, v49\n"
    "v_permlane16_swap_b32 v34, v50\n"
    "v_permlane16_swap_b32 v35, v51\n"
    "v_permlane16_swap_b32 v36, v52\n"
    "v_permlane16_swap_b32 v37, v53\n"
    "v_permlane16_swap_b32 v38, v54\n"
    "v_permlane16_swap_b32 v39, v55\n"
    "v_permlane16_swap_b32 v40, v56\n"
    "v_permlane16_swap_b32 v41, v57\n"
    "v_permlane16_swap_b32 v42, v58\n"
    "v_permlane16_swap_b32 v43, v59\n"
    "v_permlane16_swap_b32 v44, v60\n"
    "v_permlane16_swap_b32 v45, v61\n"
    "v_permlane16_swap_b32 v46, v62\n"
    "v_permlane16_swap_b32 v47, v63\n"
    "s_add_u32 s88, s88, 16384\n"
    "s_and_b32 s88, s88, 0x1c000\n"
    "s_add_u32 s82, s89, s88\n"
    "s_add_u32 s93, s92, 65536\n"
    "s_mov_b32 m0, s92\n"
    "s_nop 0\n"
    "buffer_load_dwordx4 v20, s[40:43], s82 offen offset:0 nt lds\n"
    "buffer_load_dwordx4 v21, s[40:43], s82 offen offset:1024 nt lds\n"
    "buffer_load_dwordx4 v22, s[40:43], s82 offen offset:2048 nt lds\n"
    "buffer_load_dwordx4 v23, s[40:43], s82 offen offset:3072 nt lds\n"
    "s_add_u32 s92, s92, 16384\n"
    "s_and_b32 s92, s92, 0xffff\n"
    "v_cmp_gt_u32 vcc, 8, v0\n"
    "v_lshlrev_b32 v1, 7, v0\n"
    "v_bfrev_b32 v2, 1\n"
    "v_cndmask_b32 v1, v2, v1, vcc\n"
    "s_lshl_b32 s78, s67, 10\n"
    "s_add_u32 s78, s78, 16777280\n"
    "buffer_load_dword v3, v1, s[56:59], s78 offen sc1\n"
    "v_accvgpr_write_b32 a128, 0\n"
    "v_accvgpr_write_b32 a129, 0\n"
    "v_accvgpr_write_b32 a130, 0\n"
    "v_accvgpr_write_b32 a131, 0\n"
    "v_accvgpr_write_b32 a132, 0\n"
    "v_accvgpr_write_b32 a133, 0\n"
    "v_accvgpr_write_b32 a134, 0\n"
    "v_accvgpr_write_b32 a135, 0\n"
    "v_accvgpr_write_b32 a136, 0\n"
    "v_accvgpr_write_b32 a137, 0\n"
    "v_accvgpr_write_b32 a138, 0\n"
    "v_accvgpr_write_b32 a139, 0\n"
    "v_accvgpr_write_b32 a140, 0\n"
    "v_accvgpr_write_b32 a141, 0\n"
    "v_accvgpr_write_b32 a142, 0\n"
    "v_accvgpr_write_b32 a143, 0\n"
    "v_accvgpr_write_b32 a144, 0\n"
    "v_accvgpr_write_b32 a145, 0\n"
    "v_accvgpr_write_b32 a146, 0\n"
    "v_accvgpr_write_b32 a147, 0\n"
    "v_accvgpr_write_b32 a148, 0\n"
    "v_accvgpr_write_b32 a149, 0\n"
    "v_accvgpr_write_b32 a150, 0\n"
    "v_accvgpr_write_b32 a151, 0\n"
    "v_accvgpr_write_b32 a152, 0\n"
    "v_accvgpr_write_b32 a153, 0\n"
    "v_accvgpr_write_b32 a154, 0\n"
    "v_accvgpr_write_b32 a155, 0\n"
    "v_accvgpr_write_b32 a156, 0\n"
    "v_accvgpr_write_b32 a157, 0\n"
    "v_accvgpr_write_b32 a158, 0\n"
    "v_accvgpr_write_b32 a159, 0\n"
    "v_accvgpr_write_b32 a160, 0\n"
    "v_accvgpr_write_b32 a161, 0\n"
    "v_accvgpr_write_b32 a162, 0\n"
    "v_accvgpr_write_b32 a163, 0\n"
    "v_accvgpr_write_b32 a164, 0\n"
    "v_accvgpr_write_b32 a165, 0\n"
    "v_accvgpr_write_b32 a166, 0\n"
    "v_accvgpr_write_b32 a167, 0\n"
    "v_accvgpr_write_b32 a168, 0\n"
    "v_accvgpr_write_b32 a169, 0\n"
    "v_accvgpr_write_b32 a170, 0\n"
    "v_accvgpr_write_b32 a171, 0\n"
    "v_accvgpr_write_b32 a172, 0\n"
    "v_accvgpr_write_b32 a173, 0\n"
    "v_accvgpr_write_b32 a174, 0\n"
    "v_accvgpr_write_b32 a175, 0\n"
    "v_accvgpr_write_b32 a176, 0\n"
    "v_accvgpr_write_b32 a177, 0\n"
    "v_accvgpr_write_b32 a178, 0\n"
    "v_accvgpr_write_b32 a179, 0\n"
    "v_accvgpr_write_b32 a180, 0\n"
    "v_accvgpr_write_b32 a181, 0\n"
    "v_accvgpr_write_b32 a182, 0\n"
    "v_accvgpr_write_b32 a183, 0\n"
    "v_accvgpr_write_b32 a184, 0\n"
    "v_accvgpr_write_b32 a185, 0\n"
    "v_accvgpr_write_b32 a186, 0\n"
    "v_accvgpr_write_b32 a187, 0\n"
    "v_accvgpr_write_b32 a188, 0\n"
    "v_accvgpr_write_b32 a189, 0\n"
    "v_accvgpr_write_b32 a190, 0\n"
    "v_accvgpr_write_b32 a191, 0\n"
    "s_mov_b32 s87, 1\n"
    "s_branch Lunit_%=\n"
    "Lostart_%=:\n"
    "s_add_u32 s85, s85, s90\n"
    "s_and_b32 s85, s85, s91\n"
    "buffer_load_dwordx4 v[160:163], v48, s[48:51], s85 offen\n"
    "buffer_load_dwordx4 v[164:167], v49, s[48:51], s85 offen\n"
    "buffer_load_dwordx4 v[168:171], v50, s[48:51], s85 offen\n"
    "buffer_load_dwordx4 v[172:175], v51, s[48:51], s85 offen\n"
    "buffer_load_dwordx4 v[176:179], v52, s[48:51], s85 offen\n"
    "buffer_load_dwordx4 v[180:183], v53, s[48:51], s85 offen\n"
    "buffer_load_dwordx4 v[184:187], v54, s[48:51], s85 offen\n"
    "buffer_load_dwordx4 v[188:191], v55, s[48:51], s85 offen\n"
    "s_add_u32 s85, s85, s90\n"
    "s_and_b32 s85, s85, s91\n"
    "s_add_u32 s85, s85, s90\n"
    "s_and_b32 s85, s85, s91\n"
    "s_mov_b32 s83, 0\n"
    "s_mov_b32 s71, 0\n"
    "s_mov_b32 s70, 0\n"
    "v_subrev_u32 v7, 98304, v7\n"
    "s_lshl_b32 s86, s65, 6\n"
    "v_xor_b32 v1, s86, v7\n"
    "v_xor_b32 v2, s86, v6\n"
    "s_add_u32 s86, s86, 64\n"
    "s_and_b32 s86, s86, 0x3c0\n"
    "s_waitcnt lgkmcnt(0)\n"
    "s_barrier\n"
    "ds_read_b128 a[0:3], v1 offset:0\n"
    "ds_read_b128 a[4:7], v1 offset:16384\n"
    "ds_read_b128 a[8:11], v2 offset:0\n"
    "ds_read_b128 a[12:15], v2 offset:16384\n"
    "v_pk_mov_b32 v[64:65], 0, 0\n"
    "v_pk_mov_b32 v[66:67], 0, 0\n"
    "v_pk_mov_b32 v[68:69], 0, 0\n"
    "v_pk_mov_b32 v[70:71], 0, 0\n"
    "v_pk_mov_b32 v[72:73], 0, 0\n"
    "v_pk_mov_b32 v[74:75], 0, 0\n"
    "v_pk_mov_b32 v[76:77], 0, 0\n"
    "v_pk_mov_b32 v[78:79], 0, 0\n"
    "v_pk_mov_b32 v[80:81], 0, 0\n"
    "v_pk_mov_b32 v[82:83], 0, 0\n"
    "v_pk_mov_b32 v[84:85], 0, 0\n"
    "v_pk_mov_b32 v[86:87], 0, 0\n"
    "v_pk_mov_b32 v[88:89], 0, 0\n"
    "v_pk_mov_b32 v[90:91], 0, 0\n"
    "v_pk_mov_b32 v[92:93], 0, 0\n"
    "v_pk_mov_b32 v[94:95], 0, 0\n"
    "v_pk_mov_b32 v[96:97], 0, 0\n"
    "v_pk_mov_b32 v[98:99], 0, 0\n"
    "v_pk_mov_b32 v[100:101], 0, 0\n"
    "v_pk_mov_b32 v[102:103], 0, 0\n"
    "v_pk_mov_b32 v[104:105], 0, 0\n"
    "v_pk_mov_b32 v[106:107], 0, 0\n"
    "v_pk_mov_b32 v[108:109], 0, 0\n"
    "v_pk_mov_b32 v[110:111], 0, 0\n"
    "v_pk_mov_b32 v[112:113], 0, 0\n"
    "v_pk_mov_b32 v[114:115], 0, 0\n"
    "v_pk_mov_b32 v[116:117], 0, 0\n"
    "v_pk_mov_b32 v[118:119], 0, 0\n"
    "v_pk_mov_b32 v[120:121], 0, 0\n"
    "v_pk_mov_b32 v[122:123], 0, 0\n"
    "v_pk_mov_b32 v[124:125], 0, 0\n"
    "v_pk_mov_b32 v[126:127], 0, 0\n"
    "s_waitcnt vmcnt(0)\n"
    "v_perm_b32 v32, v132, v128, v62\n"
    "v_perm_b32 v33, v140, v136, v62\n"
    "v_perm_b32 v34, v148, v144, v62\n"
    "v_perm_b32 v35, v156, v152, v62\n"
    "v_perm_b32 v36, v132, v128, v63\n"
    "s_waitcnt lgkmcnt(0)\n"
    "s_branch Lq_%=\n"
    "Lfin_%=:\n"
    "s_nop 7\n"
    "s_mul_i32 s94, s67, 32768\n"
    "s_mul_i32 s79, s64, 4096\n"
    "s_add_u32 s94, s94, s79\n"
    "s_mul_i32 s79, s68, 1024\n"
    "s_add_u32 s94, s94, s79\n"
    "v_lshlrev_b32 v1, 4, v0\n"
    "s_lshl_b32 s86, s68, 1\n"
    "v_cvt_pk_bf16_f32 v128, v64, v72\n"
    "v_cvt_pk_bf16_f32 v129, v80, v88\n"
    "v_cvt_pk_bf16_f32 v130, v96, v104\n"
    "v_cvt_pk_bf16_f32 v131, v112, v120\n"
    "v_cvt_pk_bf16_f32 v132, v65, v73\n"
    "v_cvt_pk_bf16_f32 v133, v81, v89\n"
    "v_cvt_pk_bf16_f32 v134, v97, v105\n"
    "v_cvt_pk_bf16_f32 v135, v113, v121\n"
    "v_cvt_pk_bf16_f32 v136, v66, v74\n"
    "v_cvt_pk_bf16_f32 v137, v82, v90\n"
    "v_cvt_pk_bf16_f32 v138, v98, v106\n"
    "v_cvt_pk_bf16_f32 v139, v114, v122\n"
    "v_cvt_pk_bf16_f32 v140, v67, v75\n"
    "v_cvt_pk_bf16_f32 v141, v83, v91\n"
    "v_cvt_pk_bf16_f32 v142, v99, v107\n"
    "v_cvt_pk_bf16_f32 v143, v115, v123\n"
    "s_add_u32 s78, s86, 0\n"
    "s_cmp_eq_u32 s78, s64\n"
    "s_cbranch_scc1 Lown10_%=\n"
    "s_add_u32 s78, s94, 8388608\n"
    "buffer_store_dwordx4 v[128:131], v1, s[56:59], s78 offen sc1\n"
    "s_add_u32 s78, s94, 9437184\n"
    "buffer_store_dwordx4 v[132:135], v1, s[56:59], s78 offen sc1\n"
    "s_add_u32 s78, s94, 10485760\n"
    "buffer_store_dwordx4 v[136:139], v1, s[56:59], s78 offen sc1\n"
    "s_add_u32 s78, s94, 11534336\n"
    "buffer_store_dwordx4 v[140:143], v1, s[56:59], s78 offen sc1\n"
    "s_branch Lownd10_%=\n"
    "Lown10_%=:\n"
    "ds_write_b128 v1, v[128:131] offset:36864\n"
    "ds_write_b128 v1, v[132:135] offset:37888\n"
    "ds_write_b128 v1, v[136:139] offset:38912\n"
    "ds_write_b128 v1, v[140:143] offset:39936\n"
    "Lownd10_%=:\n"
    "v_cvt_pk_bf16_f32 v144, v68, v76\n"
    "v_cvt_pk_bf16_f32 v145, v84, v92\n"
    "v_cvt_pk_bf16_f32 v146, v100, v108\n"
    "v_cvt_pk_bf16_f32 v147, v116, v124\n"
    "v_cvt_pk_bf16_f32 v148, v69, v77\n"
    "v_cvt_pk_bf16_f32 v149, v85, v93\n"
    "v_cvt_pk_bf16_f32 v150, v101, v109\n"
    "v_cvt_pk_bf16_f32 v151, v117, v125\n"
    "v_cvt_pk_bf16_f32 v152, v70, v78\n"
    "v_cvt_pk_bf16_f32 v153, v86, v94\n"
    "v_cvt_pk_bf16_f32 v154, v102, v110\n"
    "v_cvt_pk_bf16_f32 v155, v118, v126\n"
    "v_cvt_pk_bf16_f32 v156, v71, v79\n"
    "v_cvt_pk_bf16_f32 v157, v87, v95\n"
    "v_cvt_pk_bf16_f32 v158, v103, v111\n"
    "v_cvt_pk_bf16_f32 v159, v119, v127\n"
    "s_add_u32 s78, s86, 1\n"
    "s_cmp_eq_u32 s78, s64\n"
    "s_cbranch_scc1 Lown11_%=\n"
    "s_add_u32 s78, s94, 12582912\n"
    "buffer_store_dwordx4 v[144:147], v1, s[56:59], s78 offen sc1\n"
    "s_add_u32 s78, s94, 13631488\n"
    "buffer_store_dwordx4 v[148:151], v1, s[56:59], s78 offen sc1\n"
    "s_add_u32 s78, s94, 14680064\n"
    "buffer_store_dwordx4 v[152:155], v1, s[56:59], s78 offen sc1\n"
    "s_add_u32 s78, s94, 15728640\n"
    "buffer_store_dwordx4 v[156:159], v1, s[56:59], s78 offen sc1\n"
    "s_branch Lownd11_%=\n"
    "Lown11_%=:\n"
    "ds_write_b128 v1, v[144:147] offset:36864\n"
    "ds_write_b128 v1, v[148:151] offset:37888\n"
    "ds_write_b128 v1, v[152:155] offset:38912\n"
    "ds_write_b128 v1, v[156:159] offset:39936\n"
    "Lownd11_%=:\n"
    "v_accvgpr_read_b32 v192, a128\n"
    "v_accvgpr_read_b32 v193, a136\n"
    "v_accvgpr_read_b32 v194, a144\n"
    "v_accvgpr_read_b32 v195, a152\n"
    "v_accvgpr_read_b32 v196, a160\n"
    "v_accvgpr_read_b32 v197, a168\n"
    "v_accvgpr_read_b32 v198, a176\n"
    "v_accvgpr_read_b32 v199, a184\n"
    "v_cvt_pk_bf16_f32 v160, v192, v193\n"
    "v_cvt_pk_bf16_f32 v161, v194, v195\n"
    "v_cvt_pk_bf16_f32 v162, v196, v197\n"
    "v_cvt_pk_bf16_f32 v163, v198, v199\n"
    "v_accvgpr_read_b32 v200, a129\n"
    "v_accvgpr_read_b32 v201, a137\n"
    "v_accvgpr_read_b32 v202, a145\n"
    "v_accvgpr_read_b32 v203, a153\n"
    "v_accvgpr_read_b32 v204, a161\n"
    "v_accvgpr_read_b32 v205, a169\n"
    "v_accvgpr_read_b32 v206, a177\n"
    "v_accvgpr_read_b32 v207, a185\n"
    "v_cvt_pk_bf16_f32 v164, v200, v201\n"
    "v_cvt_pk_bf16_f32 v165, v202, v203\n"
    "v_cvt_pk_bf16_f32 v166, v204, v205\n"
    "v_cvt_pk_bf16_f32 v167, v206, v207\n"
    "v_accvgpr_read_b32 v192, a130\n"
    "v_accvgpr_read_b32 v193, a138\n"
    "v_accvgpr_read_b32 v194, a146\n"
    "v_accvgpr_read_b32 v195, a154\n"
    "v_accvgpr_read_b32 v196, a162\n"
    "v_accvgpr_read_b32 v197, a170\n"
    "v_accvgpr_read_b32 v198, a178\n"
    "v_accvgpr_read_b32 v199, a186\n"
    "v_cvt_pk_bf16_f32 v168, v192, v193\n"
    "v_cvt_pk_bf16_f32 v169, v194, v195\n"
    "v_cvt_pk_bf16_f32 v170, v196, v197\n"
    "v_cvt_pk_bf16_f32 v171, v198, v199\n"
    "v_accvgpr_read_b32 v200, a131\n"
    "v_accvgpr_read_b32 v201, a139\n"
    "v_accvgpr_read_b32 v202, a147\n"
    "v_accvgpr_read_b32 v203, a155\n"
    "v_accvgpr_read_b32 v204, a163\n"
    "v_accvgpr_read_b32 v205, a171\n"
    "v_accvgpr_read_b32 v206, a179\n"
    "v_accvgpr_read_b32 v207, a187\n"
    "v_cvt_pk_bf16_f32 v172, v200, v201\n"
    "v_cvt_pk_bf16_f32 v173, v202, v203\n"
    "v_cvt_pk_bf16_f32 v174, v204, v205\n"
    "v_cvt_pk_bf16_f32 v175, v206, v207\n"
    "s_add_u32 s78, s86, 0\n"
    "s_cmp_eq_u32 s78, s64\n"
    "s_cbranch_scc1 Lown00_%=\n"
    "s_add_u32 s78, s94, 0\n"
    "buffer_store_dwordx4 v[160:163], v1, s[56:59], s78 offen sc1\n"
    "s_add_u32 s78, s94, 1048576\n"
    "buffer_store_dwordx4 v[164:167], v1, s[56:59], s78 offen sc1\n"
    "s_add_u32 s78, s94, 2097152\n"
    "buffer_store_dwordx4 v[168:171], v1, s[56:59], s78 offen sc1\n"
    "s_add_u32 s78, s94, 3145728\n"
    "buffer_store_dwordx4 v[172:175], v1, s[56:59], s78 offen sc1\n"
    "s_branch Lownd00_%=\n"
    "Lown00_%=:\n"
    "ds_write_b128 v1, v[160:163] offset:32768\n"
    "ds_write_b128 v1, v[164:167] offset:33792\n"
    "ds_write_b128 v1, v[168:171] offset:34816\n"
    "ds_write_b128 v1, v[172:175] offset:35840\n"
    "Lownd00_%=:\n"
    "v_accvgpr_read_b32 v192, a132\n"
    "v_accvgpr_read_b32 v193, a140\n"
    "v_accvgpr_read_b32 v194, a148\n"
    "v_accvgpr_read_b32 v195, a156\n"
    "v_accvgpr_read_b32 v196, a164\n"
    "v_accvgpr_read_b32 v197, a172\n"
    "v_accvgpr_read_b32 v198, a180\n"
    "v_accvgpr_read_b32 v199, a188\n"
    "v_cvt_pk_bf16_f32 v176, v192, v193\n"
    "v_cvt_pk_bf16_f32 v177, v194, v195\n"
    "v_cvt_pk_bf16_f32 v178, v196, v197\n"
    "v_cvt_pk_bf16_f32 v179, v198, v199\n"
    "v_accvgpr_read_b32 v200, a133\n"
    "v_accvgpr_read_b32 v201, a141\n"
    "v_accvgpr_read_b32 v202, a149\n"
    "v_accvgpr_read_b32 v203, a157\n"
    "v_accvgpr_read_b32 v204, a165\n"
    "v_accvgpr_read_b32 v205, a173\n"
    "v_accvgpr_read_b32 v206, a181\n"
    "v_accvgpr_read_b32 v207, a189\n"
    "v_cvt_pk_bf16_f32 v180, v200, v201\n"
    "v_cvt_pk_bf16_f32 v181, v202, v203\n"
    "v_cvt_pk_bf16_f32 v182, v204, v205\n"
    "v_cvt_pk_bf16_f32 v183, v206, v207\n"
    "v_accvgpr_read_b32 v192, a134\n"
    "v_accvgpr_read_b32 v193, a142\n"
    "v_accvgpr_read_b32 v194, a150\n"
    "v_accvgpr_read_b32 v195, a158\n"
    "v_accvgpr_read_b32 v196, a166\n"
    "v_accvgpr_read_b32 v197, a174\n"
    "v_accvgpr_read_b32 v198, a182\n"
    "v_accvgpr_read_b32 v199, a190\n"
    "v_cvt_pk_bf16_f32 v184, v192, v193\n"
    "v_cvt_pk_bf16_f32 v185, v194, v195\n"
    "v_cvt_pk_bf16_f32 v186, v196, v197\n"
    "v_cvt_pk_bf16_f32 v187, v198, v199\n"
    "v_accvgpr_read_b32 v200, a135\n"
    "v_accvgpr_read_b32 v201, a143\n"
    "v_accvgpr_read_b32 v202, a151\n"
    "v_accvgpr_read_b32 v203, a159\n"
    "v_accvgpr_read_b32 v204, a167\n"
    "v_accvgpr_read_b32 v205, a175\n"
    "v_accvgpr_read_b32 v206, a183\n"
    "v_accvgpr_read_b32 v207, a191\n"
    "v_cvt_pk_bf16_f32 v188, v200, v201\n"
    "v_cvt_pk_bf16_f32 v189, v202, v203\n"
    "v_cvt_pk_bf16_f32 v190, v204, v205\n"
    "v_cvt_pk_bf16_f32 v191, v206, v207\n"
    "s_add_u32 s78, s86, 1\n"
    "s_cmp_eq_u32 s78, s64\n"
    "s_cbranch_scc1 Lown01_%=\n"
    "s_add_u32 s78, s94, 4194304\n"
    "buffer_store_dwordx4 v[176:179], v1, s[56:59], s78 offen sc1\n"
    "s_add_u32 s78, s94, 5242880\n"
    "buffer_store_dwordx4 v[180:183], v1, s[56:59], s78 offen sc1\n"
    "s_add_u32 s78, s94, 6291456\n"
    "buffer_store_dwordx4 v[184:187], v1, s[56:59], s78 offen sc1\n"
    "s_add_u32 s78, s94, 7340032\n"
    "buffer_store_dwordx4 v[188:191], v1, s[56:59], s78 offen sc1\n"
    "s_branch Lownd01_%=\n"
    "Lown01_%=:\n"
    "ds_write_b128 v1, v[176:179] offset:32768\n"
    "ds_write_b128 v1, v[180:183] offset:33792\n"
    "ds_write_b128 v1, v[184:187] offset:34816\n"
    "ds_write_b128 v1, v[188:191] offset:35840\n"
    "Lownd01_%=:\n"
    "v_readlane_b32 s86, v3, s64\n"
    "s_waitcnt vmcnt(0) lgkmcnt(0)\n"
    "s_barrier\n"
    "s_call_b64 s[98:99], Lflag_%=\n"
    "s_and_b32 s85, s64, 1\n"
    "s_mul_i32 s85, s85, 4194304\n"
    "s_mul_i32 s79, s67, 32768\n"
    "s_add_u32 s85, s85, s79\n"
    "s_lshr_b32 s79, s64, 1\n"
    "s_mul_i32 s79, s79, 1024\n"
    "s_add_u32 s85, s85, s79\n"
    "s_lshl_b32 s79, s68, 8\n"
    "s_add_u32 s85, s85, s79\n"
    "v_lshrrev_b32 v30, 4, v0\n"
    "v_lshlrev_b32 v30, 20, v30\n"
    "v_and_b32 v2, 15, v0\n"
    "v_lshl_add_u32 v30, v2, 4, v30\n"
    "v_lshrrev_b32 v1, 4, v0\n"
    "v_lshlrev_b32 v1, 10, v1\n"
    "v_and_b32 v2, 15, v0\n"
    "v_lshl_add_u32 v1, v2, 4, v1\n"
    "s_lshl_b32 s78, s68, 8\n"
    "v_add_u32 v1, s78, v1\n"
    "ds_read_b128 v[220:223], v1 offset:36864\n"
    "ds_read_b128 v[252:255], v1 offset:32768\n"
    "s_mov_b32 s72, 0x00003f80\n"
    "s_mov_b32 s73, 0x3f800000\n"
    "v_cmp_gt_u32 vcc, 8, v0\n"
    "v_lshlrev_b32 v4, 2, v0\n"
    "v_bfrev_b32 v2, 1\n"
    "v_cndmask_b32 v4, v2, v4, vcc\n"
    "s_cmp_eq_u32 s68, 0\n"
    "s_cbranch_scc0 Lpad_%=\n"
    "s_mov_b32 s83, 20000\n"
    "Lpa_%=:\n"
    "buffer_load_dword v31, v4, s[56:59], s97 offen offset:0 sc1\n"
    "s_waitcnt vmcnt(0)\n"
    "v_cmp_eq_u32 vcc, s86, v31\n"
    "s_nop 0\n"
    "s_and_b32 s79, vcc_lo, 0xff\n"
    "s_cmp_eq_u32 s79, 0xff\n"
    "s_cbranch_scc1 Lpad_%=\n"
    "s_sleep 2\n"
    "s_sub_u32 s83, s83, 1\n"
    "s_cbranch_scc0 Lpa_%=\n"
    "Lpad_%=:\n"
    "s_barrier\n"
    "v_cmp_gt_u32 vcc, 8, v0\n"
    "v_lshlrev_b32 v1, 7, v0\n"
    "v_bfrev_b32 v2, 1\n"
    "v_cndmask_b32 v1, v2, v1, vcc\n"
    "s_lshl_b32 s78, s67, 10\n"
    "s_add_u32 s78, s78, 16777280\n"
    "buffer_load_dword v31, v1, s[56:59], s78 offen sc1\n"
    "s_add_u32 s78, s64, 1\n"
    "s_and_b32 s78, s78, 7\n"
    "s_mul_i32 s78, s78, 4096\n"
    "s_add_u32 s78, s78, s85\n"
    "s_add_u32 s78, s78, 8388608\n"
    "buffer_load_dwordx4 v[192:195], v30, s[56:59], s78 offen sc1\n"
    "s_add_u32 s78, s64, 2\n"
    "s_and_b32 s78, s78, 7\n"
    "s_mul_i32 s78, s78, 4096\n"
    "s_add_u32 s78, s78, s85\n"
    "s_add_u32 s78, s78, 8388608\n"
    "buffer_load_dwordx4 v[196:199], v30, s[56:59], s78 offen sc1\n"
    "s_add_u32 s78, s64, 3\n"
    "s_and_b32 s78, s78, 7\n"
    "s_mul_i32 s78, s78, 4096\n"
    "s_add_u32 s78, s78, s85\n"
    "s_add_u32 s78, s78, 8388608\n"
    "buffer_load_dwordx4 v[200:203], v30, s[56:59], s78 offen sc1\n"
    "s_add_u32 s78, s64, 4\n"
    "s_and_b32 s78, s78, 7\n"
    "s_mul_i32 s78, s78, 4096\n"
    "s_add_u32 s78, s78, s85\n"
    "s_add_u32 s78, s78, 8388608\n"
    "buffer_load_dwordx4 v[204:207], v30, s[56:59], s78 offen sc1\n"
    "s_add_u32 s78, s64, 5\n"
    "s_and_b32 s78, s78, 7\n"
    "s_mul_i32 s78, s78, 4096\n"
    "s_add_u32 s78, s78, s85\n"
    "s_add_u32 s78, s78, 8388608\n"
    "buffer_load_dwordx4 v[208:211], v30, s[56:59], s78 offen sc1\n"
    "s_add_u32 s78, s64, 6\n"
    "s_and_b32 s78, s78, 7\n"
    "s_mul_i32 s78, s78, 4096\n"
    "s_add_u32 s78, s78, s85\n"
    "s_add_u32 s78, s78, 8388608\n"
    "buffer_load_dwordx4 v[212:215], v30, s[56:59], s78 offen sc1\n"
    "s_add_u32 s78, s64, 7\n"
    "s_and_b32 s78, s78, 7\n"
    "s_mul_i32 s78, s78, 4096\n"
    "s_add_u32 s78, s78, s85\n"
    "s_add_u32 s78, s78, 8388608\n"
    "buffer_load_dwordx4 v[216:219], v30, s[56:59], s78 offen sc1\n"
    "s_add_u32 s78, s64, 1\n"
    "s_and_b32 s78, s78, 7\n"
    "s_mul_i32 s78, s78, 4096\n"
    "s_add_u32 s78, s78, s85\n"
    "buffer_load_dwordx4 v[224:227], v30, s[56:59], s78 offen sc1\n"
    "s_add_u32 s78, s64, 2\n"
    "s_and_b32 s78, s78, 7\n"
    "s_mul_i32 s78, s78, 4096\n"
    "s_add_u32 s78, s78, s85\n"
    "buffer_load_dwordx4 v[228:231], v30, s[56:59], s78 offen sc1\n"
    "s_add_u32 s78, s64, 3\n"
    "s_and_b32 s78, s78, 7\n"
    "s_mul_i32 s78, s78, 4096\n"
    "s_add_u32 s78, s78, s85\n"
    "buffer_load_dwordx4 v[232:235], v30, s[56:59], s78 offen sc1\n"
    "s_add_u32 s78, s64, 4\n"
    "s_and_b32 s78, s78, 7\n"
    "s_mul_i32 s78, s78, 4096\n"
    "s_add_u32 s78, s78, s85\n"
    "buffer_load_dwordx4 v[236:239], v30, s[56:59], s78 offen sc1\n"
    "s_add_u32 s78, s64, 5\n"
    "s_and_b32 s78, s78, 7\n"
    "s_mul_i32 s78, s78, 4096\n"
    "s_add_u32 s78, s78, s85\n"
    "buffer_load_dwordx4 v[240:243], v30, s[56:59], s78 offen sc1\n"
    "s_add_u32 s78, s64, 6\n"
    "s_and_b32 s78, s78, 7\n"
    "s_mul_i32 s78, s78, 4096\n"
    "s_add_u32 s78, s78, s85\n"
    "buffer_load_dwordx4 v[244:247], v30, s[56:59], s78 offen sc1\n"
    "s_add_u32 s78, s64, 7\n"
    "s_and_b32 s78, s78, 7\n"
    "s_mul_i32 s78, s78, 4096\n"
    "s_add_u32 s78, s78, s85\n"
    "buffer_load_dwordx4 v[248:251], v30, s[56:59], s78 offen sc1\n"
    "s_waitcnt lgkmcnt(0)\n"
    "v_lshlrev_b32 v64, 16, v220\n"
    "v_and_b32 v65, 0xffff0000, v220\n"
    "v_lshlrev_b32 v66, 16, v221\n"
    "v_and_b32 v67, 0xffff0000, v221\n"
    "v_lshlrev_b32 v68, 16, v222\n"
    "v_and_b32 v69, 0xffff0000, v222\n"
    "v_lshlrev_b32 v70, 16, v223\n"
    "v_and_b32 v71, 0xffff0000, v223\n"
    "v_lshlrev_b32 v80, 16, v252\n"
    "v_and_b32 v81, 0xffff0000, v252\n"
    "v_lshlrev_b32 v82, 16, v253\n"
    "v_and_b32 v83, 0xffff0000, v253\n"
    "v_lshlrev_b32 v84, 16, v254\n"
    "v_and_b32 v85, 0xffff0000, v254\n"
    "v_lshlrev_b32 v86, 16, v255\n"
    "v_and_b32 v87, 0xffff0000, v255\n"
    "v_and_b32 v5, 15, v0\n"
    "v_lshlrev_b32 v5, 11, v5\n"
    "v_lshrrev_b32 v2, 4, v0\n"
    "v_lshl_add_u32 v5, v2, 4, v5\n"
    "s_lshl_b32 s85, s65, 17\n"
    "s_and_b32 s79, s64, 1\n"
    "s_lshl_b32 s79, s79, 15\n"
    "s_add_u32 s85, s85, s79\n"
    "s_lshl_b32 s79, s66, 10\n"
    "s_add_u32 s85, s85, s79\n"
    "s_lshr_b32 s79, s64, 1\n"
    "s_lshl_b32 s79, s79, 8\n"
    "s_add_u32 s85, s85, s79\n"
    "s_lshl_b32 s79, s68, 6\n"
    "s_add_u32 s85, s85, s79\n"
    "s_add_u32 s90, s85, 65536\n"
    "s_waitcnt vmcnt(14)\n"
    "v_cmp_ne_u32 vcc, v31, v3\n"
    "s_nop 0\n"
    "s_and_b32 s79, vcc_lo, 0xff\n"
    "s_cbranch_scc0 Lfresht_%=\n"
    "v_mov_b32 v3, v31\n"
    "s_mov_b32 s95, 16777216\n"
    "s_call_b64 s[98:99], Lflag_%=\n"
    "Lfresht_%=:\n"
    "s_waitcnt vmcnt(13)\n"
    "v_dot2_f32_bf16 v64, v192, s72, v64\n"
    "v_dot2_f32_bf16 v65, v192, s73, v65\n"
    "v_dot2_f32_bf16 v66, v193, s72, v66\n"
    "v_dot2_f32_bf16 v67, v193, s73, v67\n"
    "v_dot2_f32_bf16 v68, v194, s72, v68\n"
    "v_dot2_f32_bf16 v69, v194, s73, v69\n"
    "v_dot2_f32_bf16 v70, v195, s72, v70\n"
    "v_dot2_f32_bf16 v71, v195, s73, v71\n"
    "s_waitcnt vmcnt(12)\n"
    "v_dot2_f32_bf16 v64, v196, s72, v64\n"
    "v_dot2_f32_bf16 v65, v196, s73, v65\n"
    "v_dot2_f32_bf16 v66, v197, s72, v66\n"
    "v_dot2_f32_bf16 v67, v197, s73, v67\n"
    "v_dot2_f32_bf16 v68, v198, s72, v68\n"
    "v_dot2_f32_bf16 v69, v198, s73, v69\n"
    "v_dot2_f32_bf16 v70, v199, s72, v70\n"
    "v_dot2_f32_bf16 v71, v199, s73, v71\n"
    "s_waitcnt vmcnt(11)\n"
    "v_dot2_f32_bf16 v64, v200, s72, v64\n"
    "v_dot2_f32_bf16 v65, v200, s73, v65\n"
    "v_dot2_f32_bf16 v66, v201, s72, v66\n"
    "v_dot2_f32_bf16 v67, v201, s73, v67\n"
    "v_dot2_f32_bf16 v68, v202, s72, v68\n"
    "v_dot2_f32_bf16 v69, v202, s73, v69\n"
    "v_dot2_f32_bf16 v70, v203, s72, v70\n"
    "v_dot2_f32_bf16 v71, v203, s73, v71\n"
    "s_waitcnt vmcnt(10)\n"
    "v_dot2_f32_bf16 v64, v204, s72, v64\n"
    "v_dot2_f32_bf16 v65, v204, s73, v65\n"
    "v_dot2_f32_bf16 v66, v205, s72, v66\n"
    "v_dot2_f32_bf16 v67, v205, s73, v67\n"
    "v_dot2_f32_bf16 v68, v206, s72, v68\n"
    "v_dot2_f32_bf16 v69, v206, s73, v69\n"
    "v_dot2_f32_bf16 v70, v207, s72, v70\n"
    "v_dot2_f32_bf16 v71, v207, s73, v71\n"
    "s_waitcnt vmcnt(9)\n"
    "v_dot2_f32_bf16 v64, v208, s72, v64\n"
    "v_dot2_f32_bf16 v65, v208, s73, v65\n"
    "v_dot2_f32_bf16 v66, v209, s72, v66\n"
    "v_dot2_f32_bf16 v67, v209, s73, v67\n"
    "v_dot2_f32_bf16 v68, v210, s72, v68\n"
    "v_dot2_f32_bf16 v69, v210, s73, v69\n"
    "v_dot2_f32_bf16 v70, v211, s72, v70\n"
    "v_dot2_f32_bf16 v71, v211, s73, v71\n"
    "s_waitcnt vmcnt(8)\n"
    "v_dot2_f32_bf16 v64, v212, s72, v64\n"
    "v_dot2_f32_bf16 v65, v212, s73, v65\n"
    "v_dot2_f32_bf16 v66, v213, s72, v66\n"
    "v_dot2_f32_bf16 v67, v213, s73, v67\n"
    "v_dot2_f32_bf16 v68, v214, s72, v68\n"
    "v_dot2_f32_bf16 v69, v214, s73, v69\n"
    "v_dot2_f32_bf16 v70, v215, s72, v70\n"
    "v_dot2_f32_bf16 v71, v215, s73, v71\n"
    "s_waitcnt vmcnt(7)\n"
    "v_dot2_f32_bf16 v64, v216, s72, v64\n"
    "v_dot2_f32_bf16 v65, v216, s73, v65\n"
    "v_dot2_f32_bf16 v66, v217, s72, v66\n"
    "v_dot2_f32_bf16 v67, v217, s73, v67\n"
    "v_dot2_f32_bf16 v68, v218, s72, v68\n"
    "v_dot2_f32_bf16 v69, v218, s73, v69\n"
    "v_dot2_f32_bf16 v70, v219, s72, v70\n"
    "v_dot2_f32_bf16 v71, v219, s73, v71\n"
    "v_cvt_pk_bf16_f32 v64, v64, v65\n"
    "v_cvt_pk_bf16_f32 v65, v66, v67\n"
    "v_cvt_pk_bf16_f32 v66, v68, v69\n"
    "v_cvt_pk_bf16_f32 v67, v70, v71\n"
    "buffer_store_dwordx4 v[64:67], v5, s[60:63], s90 offen \n"
    "s_waitcnt vmcnt(7)\n"
    "v_dot2_f32_bf16 v80, v224, s72, v80\n"
    "v_dot2_f32_bf16 v81, v224, s73, v81\n"
    "v_dot2_f32_bf16 v82, v225, s72, v82\n"
    "v_dot2_f32_bf16 v83, v225, s73, v83\n"
    "v_dot2_f32_bf16 v84, v226, s72, v84\n"
    "v_dot2_f32_bf16 v85, v226, s73, v85\n"
    "v_dot2_f32_bf16 v86, v227, s72, v86\n"
    "v_dot2_f32_bf16 v87, v227, s73, v87\n"
    "s_waitcnt vmcnt(6)\n"
    "v_dot2_f32_bf16 v80, v228, s72, v80\n"
    "v_dot2_f32_bf16 v81, v228, s73, v81\n"
    "v_dot2_f32_bf16 v82, v229, s72, v82\n"
    "v_dot2_f32_bf16 v83, v229, s73, v83\n"
    "v_dot2_f32_bf16 v84, v230, s72, v84\n"
    "v_dot2_f32_bf16 v85, v230, s73, v85\n"
    "v_dot2_f32_bf16 v86, v231, s72, v86\n"
    "v_dot2_f32_bf16 v87, v231, s73, v87\n"
    "s_waitcnt vmcnt(5)\n"
    "v_dot2_f32_bf16 v80, v232, s72, v80\n"
    "v_dot2_f32_bf16 v81, v232, s73, v81\n"
    "v_dot2_f32_bf16 v82, v233, s72, v82\n"
    "v_dot2_f32_bf16 v83, v233, s73, v83\n"
    "v_dot2_f32_bf16 v84, v234, s72, v84\n"
    "v_dot2_f32_bf16 v85, v234, s73, v85\n"
    "v_dot2_f32_bf16 v86, v235, s72, v86\n"
    "v_dot2_f32_bf16 v87, v235, s73, v87\n"
    "s_waitcnt vmcnt(4)\n"
    "v_dot2_f32_bf16 v80, v236, s72, v80\n"
    "v_dot2_f32_bf16 v81, v236, s73, v81\n"
    "v_dot2_f32_bf16 v82, v237, s72, v82\n"
    "v_dot2_f32_bf16 v83, v237, s73, v83\n"
    "v_dot2_f32_bf16 v84, v238, s72, v84\n"
    "v_dot2_f32_bf16 v85, v238, s73, v85\n"
    "v_dot2_f32_bf16 v86, v239, s72, v86\n"
    "v_dot2_f32_bf16 v87, v239, s73, v87\n"
    "s_waitcnt vmcnt(3)\n"
    "v_dot2_f32_bf16 v80, v240, s72, v80\n"
    "v_dot2_f32_bf16 v81, v240, s73, v81\n"
    "v_dot2_f32_bf16 v82, v241, s72, v82\n"
    "v_dot2_f32_bf16 v83, v241, s73, v83\n"
    "v_dot2_f32_bf16 v84, v242, s72, v84\n"
    "v_dot2_f32_bf16 v85, v242, s73, v85\n"
    "v_dot2_f32_bf16 v86, v243, s72, v86\n"
    "v_dot2_f32_bf16 v87, v243, s73, v87\n"
    "s_waitcnt vmcnt(2)\n"
    "v_dot2_f32_bf16 v80, v244, s72, v80\n"
    "v_dot2_f32_bf16 v81, v244, s73, v81\n"
    "v_dot2_f32_bf16 v82, v245, s72, v82\n"
    "v_dot2_f32_bf16 v83, v245, s73, v83\n"
    "v_dot2_f32_bf16 v84, v246, s72, v84\n"
    "v_dot2_f32_bf16 v85, v246, s73, v85\n"
    "v_dot2_f32_bf16 v86, v247, s72, v86\n"
    "v_dot2_f32_bf16 v87, v247, s73, v87\n"
    "s_waitcnt vmcnt(1)\n"
    "v_dot2_f32_bf16 v80, v248, s72, v80\n"
    "v_dot2_f32_bf16 v81, v248, s73, v81\n"
    "v_dot2_f32_bf16 v82, v249, s72, v82\n"
    "v_dot2_f32_bf16 v83, v249, s73, v83\n"
    "v_dot2_f32_bf16 v84, v250, s72, v84\n"
    "v_dot2_f32_bf16 v85, v250, s73, v85\n"
    "v_dot2_f32_bf16 v86, v251, s72, v86\n"
    "v_dot2_f32_bf16 v87, v251, s73, v87\n"
    "v_cvt_pk_bf16_f32 v80, v80, v81\n"
    "v_cvt_pk_bf16_f32 v81, v82, v83\n"
    "v_cvt_pk_bf16_f32 v82, v84, v85\n"
    "v_cvt_pk_bf16_f32 v83, v86, v87\n"
    "buffer_store_dwordx4 v[80:83], v5, s[60:63], s85 offen \n"
    "s_branch Lend_%=\n"
    "Lq0h_%=:\n"
    "s_bitcmp1_b32 s71, s83\n"
    "s_cbranch_scc1 Lq0b_%=\n"
    "s_waitcnt vmcnt(24) lgkmcnt(0)\n"
    "s_branch Lq0w_%=\n"
    "Lq0b_%=:\n"
    "s_waitcnt vmcnt(16) lgkmcnt(0)\n"
    "s_call_b64 s[80:81], Lkvpre_%=\n"
    "s_branch Lq0w_%=\n"
    "Lq1h_%=:\n"
    "s_bitcmp1_b32 s71, s83\n"
    "s_cbranch_scc1 Lq1b_%=\n"
    "s_waitcnt vmcnt(24) lgkmcnt(0)\n"
    "s_branch Lq1w_%=\n"
    "Lq1b_%=:\n"
    "s_waitcnt vmcnt(16) lgkmcnt(0)\n"
    "s_call_b64 s[80:81], Lkvpre_%=\n"
    "s_branch Lq1w_%=\n"
    "Lq2h_%=:\n"
    "s_bitcmp1_b32 s71, s83\n"
    "s_cbranch_scc1 Lq2b_%=\n"
    "s_waitcnt vmcnt(24) lgkmcnt(0)\n"
    "s_branch Lq2w_%=\n"
    "Lq2b_%=:\n"
    "s_waitcnt vmcnt(16) lgkmcnt(0)\n"
    "s_call_b64 s[80:81], Lkvpre_%=\n"
    "s_branch Lq2w_%=\n"
    "Lq3h_%=:\n"
    "s_bitcmp1_b32 s71, s83\n"
    "s_cbranch_scc1 Lq3b_%=\n"
    "s_waitcnt vmcnt(24) lgkmcnt(0)\n"
    "s_branch Lq3w_%=\n"
    "Lq3b_%=:\n"
    "s_waitcnt vmcnt(16) lgkmcnt(0)\n"
    "s_call_b64 s[80:81], Lkvpre_%=\n"
    "s_branch Lq3w_%=\n"
    "Lqp0h_%=:\n"
    "s_bitcmp1_b32 s71, s83\n"
    "s_cbranch_scc1 Lqp0b_%=\n"
    "s_waitcnt vmcnt(24) lgkmcnt(0)\n"
    "s_branch Lqp0w_%=\n"
    "Lqp0b_%=:\n"
    "s_waitcnt vmcnt(16) lgkmcnt(0)\n"
    "s_call_b64 s[80:81], Lkvpre_%=\n"
    "s_branch Lqp0w_%=\n"
    "Lqp1h_%=:\n"
    "s_bitcmp1_b32 s71, s83\n"
    "s_cbranch_scc1 Lqp1b_%=\n"
    "s_waitcnt vmcnt(16) lgkmcnt(0)\n"
    "s_branch Lqp1w_%=\n"
    "Lqp1b_%=:\n"
    "s_waitcnt vmcnt(8) lgkmcnt(0)\n"
    "s_call_b64 s[80:81], Lkvpre_%=\n"
    "s_branch Lqp1w_%=\n"
    "Lqp2h_%=:\n"
    "s_bitcmp1_b32 s71, s83\n"
    "s_cbranch_scc1 Lqp2b_%=\n"
    "s_waitcnt vmcnt(8) lgkmcnt(0)\n"
    "s_branch Lqp2w_%=\n"
    "Lqp2b_%=:\n"
    "s_waitcnt vmcnt(0) lgkmcnt(0)\n"
    "s_call_b64 s[80:81], Lkvpre_%=\n"
    "s_branch Lqp2w_%=\n"
    "Lkvpre_%=:\n"
    "s_cmp_eq_u32 s84, 0\n"
    "s_cbranch_scc0 Lkb1_%=\n"
    "s_add_u32 s78, s35, 0\n"
    "s_and_b32 s78, s78, 0x1c000\n"
    "s_add_u32 s78, s78, s89\n"
    "s_add_u32 m0, s92, 0\n"
    "s_nop 0\n"
    "buffer_load_dwordx4 v20, s[40:43], s78 offen offset:0 nt lds\n"
    "buffer_load_dwordx4 v21, s[40:43], s78 offen offset:1024 nt lds\n"
    "buffer_load_dwordx4 v22, s[40:43], s78 offen offset:2048 nt lds\n"
    "buffer_load_dwordx4 v23, s[40:43], s78 offen offset:3072 nt lds\n"
    "s_add_u32 s78, s35, 0\n"
    "s_and_b32 s78, s78, 0x1c000\n"
    "s_add_u32 s78, s78, s89\n"
    "s_add_u32 m0, s93, 0\n"
    "s_nop 0\n"
    "buffer_load_dwordx4 v24, s[44:47], s78 offen offset:0 nt lds\n"
    "buffer_load_dwordx4 v24, s[44:47], s78 offen offset:1024 nt lds\n"
    "buffer_load_dwordx4 v24, s[44:47], s78 offen offset:2048 nt lds\n"
    "buffer_load_dwordx4 v24, s[44:47], s78 offen offset:3072 nt lds\n"
    "s_add_u32 s84, s84, 1\n"
    "s_setpc_b64 s[80:81]\n"
    "Lkb1_%=:\n"
    "s_cmp_eq_u32 s84, 1\n"
    "s_cbranch_scc0 Lkb2_%=\n"
    "s_add_u32 s78, s35, 16384\n"
    "s_and_b32 s78, s78, 0x1c000\n"
    "s_add_u32 s78, s78, s89\n"
    "s_add_u32 m0, s92, 16384\n"
    "s_nop 0\n"
    "buffer_load_dwordx4 v20, s[40:43], s78 offen offset:0 nt lds\n"
    "buffer_load_dwordx4 v21, s[40:43], s78 offen offset:1024 nt lds\n"
    "buffer_load_dwordx4 v22, s[40:43], s78 offen offset:2048 nt lds\n"
    "buffer_load_dwordx4 v23, s[40:43], s78 offen offset:3072 nt lds\n"
    "s_add_u32 s78, s35, 16384\n"
    "s_and_b32 s78, s78, 0x1c000\n"
    "s_add_u32 s78, s78, s89\n"
    "s_add_u32 m0, s93, 16384\n"
    "s_nop 0\n"
    "buffer_load_dwordx4 v24, s[44:47], s78 offen offset:0 nt lds\n"
    "buffer_load_dwordx4 v24, s[44:47], s78 offen offset:1024 nt lds\n"
    "buffer_load_dwordx4 v24, s[44:47], s78 offen offset:2048 nt lds\n"
    "buffer_load_dwordx4 v24, s[44:47], s78 offen offset:3072 nt lds\n"
    "s_add_u32 s84, s84, 1\n"
    "s_setpc_b64 s[80:81]\n"
    "Lkb2_%=:\n"
    "s_cmp_eq_u32 s84, 2\n"
    "s_cbranch_scc0 Lkb3_%=\n"
    "s_add_u32 s78, s35, 32768\n"
    "s_and_b32 s78, s78, 0x1c000\n"
    "s_add_u32 s78, s78, s89\n"
    "s_add_u32 m0, s92, 32768\n"
    "s_nop 0\n"
    "buffer_load_dwordx4 v20, s[40:43], s78 offen offset:0 nt lds\n"
    "buffer_load_dwordx4 v21, s[40:43], s78 offen offset:1024 nt lds\n"
    "buffer_load_dwordx4 v22, s[40:43], s78 offen offset:2048 nt lds\n"
    "buffer_load_dwordx4 v23, s[40:43], s78 offen offset:3072 nt lds\n"
    "s_add_u32 s78, s35, 49152\n"
    "s_and_b32 s78, s78, 0x1c000\n"
    "s_add_u32 s78, s78, s89\n"
    "s_add_u32 m0, s92, 49152\n"
    "s_nop 0\n"
    "buffer_load_dwordx4 v20, s[40:43], s78 offen offset:0 nt lds\n"
    "buffer_load_dwordx4 v21, s[40:43], s78 offen offset:1024 nt lds\n"
    "buffer_load_dwordx4 v22, s[40:43], s78 offen offset:2048 nt lds\n"
    "buffer_load_dwordx4 v23, s[40:43], s78 offen offset:3072 nt lds\n"
    "s_add_u32 s84, s84, 1\n"
    "s_setpc_b64 s[80:81]\n"
    "Lkb3_%=:\n"
    "s_barrier\n"
    "s_add_u32 s78, s35, 32768\n"
    "s_and_b32 s78, s78, 0x1c000\n"
    "s_add_u32 s78, s78, s89\n"
    "s_add_u32 m0, s93, 32768\n"
    "s_nop 0\n"
    "buffer_load_dwordx4 v24, s[44:47], s78 offen offset:0 nt lds\n"
    "buffer_load_dwordx4 v24, s[44:47], s78 offen offset:1024 nt lds\n"
    "buffer_load_dwordx4 v24, s[44:47], s78 offen offset:2048 nt lds\n"
    "buffer_load_dwordx4 v24, s[44:47], s78 offen offset:3072 nt lds\n"
    "s_add_u32 s78, s35, 49152\n"
    "s_and_b32 s78, s78, 0x1c000\n"
    "s_add_u32 s78, s78, s89\n"
    "s_add_u32 m0, s93, 49152\n"
    "s_nop 0\n"
    "buffer_load_dwordx4 v24, s[44:47], s78 offen offset:0 nt lds\n"
    "buffer_load_dwordx4 v24, s[44:47], s78 offen offset:1024 nt lds\n"
    "buffer_load_dwordx4 v24, s[44:47], s78 offen offset:2048 nt lds\n"
    "buffer_load_dwordx4 v24, s[44:47], s78 offen offset:3072 nt lds\n"
    "s_add_u32 s84, s84, 1\n"
    "s_setpc_b64 s[80:81]\n"
    "Lflag_%=:\n"
    "v_lshl_add_u32 v1, s68, 6, v0\n"
    "v_cmp_gt_u32 vcc, 8, v1\n"
    "v_lshlrev_b32 v1, 7, v0\n"
    "v_bfrev_b32 v2, 1\n"
    "v_cndmask_b32 v1, v2, v1, vcc\n"
    "s_lshl_b32 s78, s67, 10\n"
    "s_lshl_b32 s79, s64, 2\n"
    "s_add_u32 s78, s78, s79\n"
    "s_add_u32 s78, s78, s95\n"
    "buffer_store_dword v3, v1, s[56:59], s78 offen sc1\n"
    "s_setpc_b64 s[98:99]\n"
    "Lpacea_%=:\n"
    "s_cmp_gt_i32 s78, 2000\n"
    "s_cbranch_scc1 Lpacear_%=\n"
    "Lpaceal_%=:\n"
    "s_sleep 1\n"
    "s_memrealtime s[74:75]\n"
    "s_waitcnt lgkmcnt(0)\n"
    "s_sub_i32 s78, s34, s74\n"
    "s_cmp_gt_i32 s78, 0\n"
    "s_cbranch_scc1 Lpaceal_%=\n"
    "s_branch Lpacear_%=\n"
    "Lpaceb_%=:\n"
    "s_cmp_gt_i32 s78, 2000\n"
    "s_cbranch_scc1 Lpacebr_%=\n"
    "Lpacebl_%=:\n"
    "s_sleep 1\n"
    "s_memrealtime s[74:75]\n"
    "s_waitcnt lgkmcnt(0)\n"
    "s_sub_i32 s78, s34, s74\n"
    "s_cmp_gt_i32 s78, 0\n"
    "s_cbranch_scc1 Lpacebl_%=\n"
    "s_branch Lpacebr_%=\n"
    "LaltaV_%=:\n"
    "s_cmp_eq_u32 s69, 14\n"
    "s_cbranch_scc0 LaltaV14_%=\n"
    "v_lshrrev_b32 v5, 5, v0\n"
    "v_lshlrev_b32 v1, 14, v5\n"
    "v_bfe_u32 v5, v0, 4, 1\n"
    "v_lshl_add_u32 v1, v5, 12, v1\n"
    "v_and_b32 v4, 15, v0\n"
    "v_lshl_add_u32 v1, v4, 4, v1\n"
    "v_add_u32 v1, s94, v1\n"
    "s_add_u32 s78, s65, 2\n"
    "s_and_b32 s78, s78, 15\n"
    "s_lshl_b32 s78, s78, 15\n"
    "v_bfe_u32 v2, v0, 4, 1\n"
    "v_lshlrev_b32 v2, 10, v2\n"
    "v_sub_u32 v4, v1, v2\n"
    "v_add_u32 v1, v1, v2\n"
    "buffer_load_dwordx4 v[192:195], v1, s[52:55], s78 offen offset:0\n"
    "buffer_load_dwordx4 v[196:199], v4, s[52:55], s78 offen offset:1024\n"
    "buffer_load_dwordx4 v[200:203], v1, s[52:55], s78 offen offset:2048\n"
    "buffer_load_dwordx4 v[204:207], v4, s[52:55], s78 offen offset:3072\n"
    "LaltaV14_%=:\n"
    "s_branch LaV_%=\n"
    "LaltaK_%=:\n"
    "s_cmp_eq_u32 s69, 4\n"
    "s_cbranch_scc0 LaltaK4_%=\n"
    "s_add_u32 s89, s89, 1048576\n"
    "s_add_u32 s82, s82, 1048576\n"
    "buffer_load_dwordx4 v20, s[40:43], s82 offen offset:0 nt lds\n"
    "buffer_load_dwordx4 v21, s[40:43], s82 offen offset:1024 nt lds\n"
    "buffer_load_dwordx4 v22, s[40:43], s82 offen offset:2048 nt lds\n"
    "buffer_load_dwordx4 v23, s[40:43], s82 offen offset:3072 nt lds\n"
    "LaltaK4_%=:\n"
    "s_branch LaK_%=\n"
    "LaltbV_%=:\n"
    "s_cmp_eq_u32 s69, 13\n"
    "s_cbranch_scc0 LaltbV13_%=\n"
    "s_waitcnt vmcnt(4)\n"
    "s_barrier\n"
    "s_getpc_b64 s[36:37]\n"
    "Lpc2_%=:\n"
    "s_add_u32 s36, s36, Lostart_%=-Lpc2_%=\n"
    "s_addc_u32 s37, s37, -1\n"
    "s_and_b32 s37, s37, 0xffff\n"
    "s_mov_b32 s38, Lrxa_%=-Lostart_%=\n"
    "v_cmp_gt_u32 vcc, 16, v0\n"
    "v_lshlrev_b32 v1, 7, v0\n"
    "v_bfrev_b32 v2, 1\n"
    "v_cndmask_b32 v1, v2, v1, vcc\n"
    "s_lshl_b32 s78, s68, 11\n"
    "buffer_load_dword v223, v1, s[36:39], s78 offen\n"
    "v_lshlrev_b32 v2, 7, v0\n"
    "s_add_u32 s79, s65, 2\n"
    "s_and_b32 s79, s79, 15\n"
    "s_lshl_b32 s79, s79, 15\n"
    "s_lshl_b32 s78, s64, 19\n"
    "s_add_u32 s78, s78, s79\n"
    "s_lshl_b32 s79, s68, 13\n"
    "s_add_u32 s78, s78, s79\n"
    "buffer_load_dword v222, v2, s[52:55], s78 offen\n"
    "LaltbV13_%=:\n"
    "s_branch LbV_%=\n"
    "LaltbK_%=:\n"
    "s_branch LbK_%=\n"
    "Lrxa_%=:\n"
    "v_and_b32 v1, 31, v0\n"
    "v_lshlrev_b32 v1, 8, v1\n"
    "v_lshrrev_b32 v2, 5, v0\n"
    "v_lshl_add_u32 v1, v2, 7, v1\n"
    "s_lshl_b32 s78, s65, 4\n"
    "s_add_u32 s78, s78, s64\n"
    "s_lshl_b32 s78, s78, 17\n"
    "s_lshl_b32 s79, s87, 20\n"
    "s_add_u32 s78, s78, s79\n"
    "s_lshl_b32 s79, s69, 14\n"
    "s_add_u32 s79, s79, s35\n"
    "s_and_b32 s79, s79, 0x1c000\n"
    "s_add_u32 s78, s78, s79\n"
    "s_nop 7\n"
    "s_waitcnt lgkmcnt(0)\n"
    "buffer_load_dwordx4 a[0:3], v1, s[40:43], s78 offen offset:0\n"
    "buffer_load_dwordx4 a[4:7], v1, s[40:43], s78 offen offset:16\n"
    "buffer_load_dwordx4 a[8:11], v1, s[40:43], s78 offen offset:32\n"
    "buffer_load_dwordx4 a[12:15], v1, s[40:43], s78 offen offset:48\n"
    "buffer_load_dwordx4 a[16:19], v1, s[40:43], s78 offen offset:64\n"
    "buffer_load_dwordx4 a[20:23], v1, s[40:43], s78 offen offset:80\n"
    "buffer_load_dwordx4 a[24:27], v1, s[40:43], s78 offen offset:96\n"
    "buffer_load_dwordx4 a[28:31], v1, s[40:43], s78 offen offset:112\n"
    "s_add_u32 s78, s78, 8192\n"
    "buffer_load_dwordx4 a[32:35], v1, s[40:43], s78 offen offset:0\n"
    "buffer_load_dwordx4 a[36:39], v1, s[40:43], s78 offen offset:16\n"
    "buffer_load_dwordx4 a[40:43], v1, s[40:43], s78 offen offset:32\n"
    "buffer_load_dwordx4 a[44:47], v1, s[40:43], s78 offen offset:48\n"
    "buffer_load_dwordx4 a[48:51], v1, s[40:43], s78 offen offset:64\n"
    "buffer_load_dwordx4 a[52:55], v1, s[40:43], s78 offen offset:80\n"
    "buffer_load_dwordx4 a[56:59], v1, s[40:43], s78 offen offset:96\n"
    "buffer_load_dwordx4 a[60:63], v1, s[40:43], s78 offen offset:112\n"
    "s_waitcnt vmcnt(0)\n"
    "v_mfma_f32_32x32x16_bf16 v[128:143], a[0:3], v[32:35], 0\n"
    "v_mfma_f32_32x32x16_bf16 v[128:143], a[4:7], v[36:39], v[128:143]\n"
    "v_mfma_f32_32x32x16_bf16 v[128:143], a[8:11], v[40:43], v[128:143]\n"
    "v_mfma_f32_32x32x16_bf16 v[128:143], a[12:15], v[44:47], v[128:143]\n"
    "v_mfma_f32_32x32x16_bf16 v[128:143], a[16:19], v[48:51], v[128:143]\n"
    "v_mfma_f32_32x32x16_bf16 v[128:143], a[20:23], v[52:55], v[128:143]\n"
    "v_mfma_f32_32x32x16_bf16 v[128:143], a[24:27], v[56:59], v[128:143]\n"
    "v_mfma_f32_32x32x16_bf16 v[128:143], a[28:31], v[60:63], v[128:143]\n"
    "v_mfma_f32_32x32x16_bf16 v[144:159], a[32:35], v[32:35], 0\n"
    "v_mfma_f32_32x32x16_bf16 v[144:159], a[36:39], v[36:39], v[144:159]\n"
    "v_mfma_f32_32x32x16_bf16 v[144:159], a[40:43], v[40:43], v[144:159]\n"
    "v_mfma_f32_32x32x16_bf16 v[144:159], a[44:47], v[44:47], v[144:159]\n"
    "v_mfma_f32_32x32x16_bf16 v[144:159], a[48:51], v[48:51], v[144:159]\n"
    "v_mfma_f32_32x32x16_bf16 v[144:159], a[52:55], v[52:55], v[144:159]\n"
    "v_mfma_f32_32x32x16_bf16 v[144:159], a[56:59], v[56:59], v[144:159]\n"
    "v_mfma_f32_32x32x16_bf16 v[144:159], a[60:63], v[60:63], v[144:159]\n"
    "s_nop 3\n"
    "v_max3_f32 v26, v128, v129, v130\n"
    "v_max3_f32 v28, v131, v132, v133\n"
    "v_max3_f32 v26, v26, v134, v135\n"
    "v_max3_f32 v28, v28, v136, v137\n"
    "v_max3_f32 v26, v26, v138, v139\n"
    "v_max3_f32 v28, v28, v140, v141\n"
    "v_max3_f32 v26, v26, v142, v143\n"
    "s_nop 0\n"
    "v_max3_f32 v28, v28, v144, v145\n"
    "v_max3_f32 v26, v26, v146, v147\n"
    "v_max3_f32 v28, v28, v148, v149\n"
    "v_max3_f32 v26, v26, v150, v151\n"
    "v_max3_f32 v28, v28, v152, v153\n"
    "v_max3_f32 v26, v26, v154, v155\n"
    "v_max3_f32 v28, v28, v156, v157\n"
    "v_max3_f32 v26, v26, v158, v159\n"
    "v_max_f32 v26, v26, v28\n"
    "v_mov_b32 v28, v26\n"
    "s_nop 1\n"
    "v_permlane32_swap_b32 v26, v28\n"
    "v_max_f32 v26, v26, v28\n"
    "v_max_f32 v26, v26, -v29\n"
    "v_add_f32 v28, v26, v29\n"
    "v_sub_f32 v29, 0, v26\n"
    "v_exp_f32 v30, -v28\n"
    "v_sub_f32 v128, v128, v26\n"
    "v_sub_f32 v129, v129, v26\n"
    "v_sub_f32 v130, v130, v26\n"
    "v_sub_f32 v131, v131, v26\n"
    "v_sub_f32 v132, v132, v26\n"
    "v_sub_f32 v133, v133, v26\n"
    "v_sub_f32 v134, v134, v26\n"
    "v_sub_f32 v135, v135, v26\n"
    "v_sub_f32 v136, v136, v26\n"
    "v_sub_f32 v137, v137, v26\n"
    "v_sub_f32 v138, v138, v26\n"
    "v_sub_f32 v139, v139, v26\n"
    "v_sub_f32 v140, v140, v26\n"
    "v_sub_f32 v141, v141, v26\n"
    "v_sub_f32 v142, v142, v26\n"
    "v_sub_f32 v143, v143, v26\n"
    "v_sub_f32 v144, v144, v26\n"
    "v_sub_f32 v145, v145, v26\n"
    "v_sub_f32 v146, v146, v26\n"
    "v_sub_f32 v147, v147, v26\n"
    "v_sub_f32 v148, v148, v26\n"
    "v_sub_f32 v149, v149, v26\n"
    "v_sub_f32 v150, v150, v26\n"
    "v_sub_f32 v151, v151, v26\n"
    "v_sub_f32 v152, v152, v26\n"
    "v_sub_f32 v153, v153, v26\n"
    "v_sub_f32 v154, v154, v26\n"
    "v_sub_f32 v155, v155, v26\n"
    "v_sub_f32 v156, v156, v26\n"
    "v_sub_f32 v157, v157, v26\n"
    "v_sub_f32 v158, v158, v26\n"
    "v_sub_f32 v159, v159, v26\n"
    "v_sub_f32 v224, v224, v28\n"
    "v_sub_f32 v225, v225, v28\n"
    "v_sub_f32 v226, v226, v28\n"
    "v_sub_f32 v227, v227, v28\n"
    "v_sub_f32 v228, v228, v28\n"
    "v_sub_f32 v229, v229, v28\n"
    "v_sub_f32 v230, v230, v28\n"
    "v_sub_f32 v231, v231, v28\n"
    "v_sub_f32 v232, v232, v28\n"
    "v_sub_f32 v233, v233, v28\n"
    "v_sub_f32 v234, v234, v28\n"
    "v_sub_f32 v235, v235, v28\n"
    "v_sub_f32 v236, v236, v28\n"
    "v_sub_f32 v237, v237, v28\n"
    "v_sub_f32 v238, v238, v28\n"
    "v_sub_f32 v239, v239, v28\n"
    "v_sub_f32 v240, v240, v28\n"
    "v_sub_f32 v241, v241, v28\n"
    "v_sub_f32 v242, v242, v28\n"
    "v_sub_f32 v243, v243, v28\n"
    "v_sub_f32 v244, v244, v28\n"
    "v_sub_f32 v245, v245, v28\n"
    "v_sub_f32 v246, v246, v28\n"
    "v_sub_f32 v247, v247, v28\n"
    "v_sub_f32 v248, v248, v28\n"
    "v_sub_f32 v249, v249, v28\n"
    "v_sub_f32 v250, v250, v28\n"
    "v_sub_f32 v251, v251, v28\n"
    "v_sub_f32 v252, v252, v28\n"
    "v_sub_f32 v253, v253, v28\n"
    "v_sub_f32 v254, v254, v28\n"
    "v_sub_f32 v255, v255, v28\n"
    "v_mov_b32 v160, v29\n"
    "v_mov_b32 v161, v29\n"
    "v_mov_b32 v162, v29\n"
    "v_mov_b32 v163, v29\n"
    "v_mov_b32 v164, v29\n"
    "v_mov_b32 v165, v29\n"
    "v_mov_b32 v166, v29\n"
    "v_mov_b32 v167, v29\n"
    "v_mov_b32 v168, v29\n"
    "v_mov_b32 v169, v29\n"
    "v_mov_b32 v170, v29\n"
    "v_mov_b32 v171, v29\n"
    "v_mov_b32 v172, v29\n"
    "v_mov_b32 v173, v29\n"
    "v_mov_b32 v174, v29\n"
    "v_mov_b32 v175, v29\n"
    "v_pk_mul_f32 v[64:65], v[64:65], v[30:31] op_sel_hi:[1,0]\n"
    "v_pk_mul_f32 v[66:67], v[66:67], v[30:31] op_sel_hi:[1,0]\n"
    "v_pk_mul_f32 v[68:69], v[68:69], v[30:31] op_sel_hi:[1,0]\n"
    "v_pk_mul_f32 v[70:71], v[70:71], v[30:31] op_sel_hi:[1,0]\n"
    "v_pk_mul_f32 v[72:73], v[72:73], v[30:31] op_sel_hi:[1,0]\n"
    "v_pk_mul_f32 v[74:75], v[74:75], v[30:31] op_sel_hi:[1,0]\n"
    "v_pk_mul_f32 v[76:77], v[76:77], v[30:31] op_sel_hi:[1,0]\n"
    "v_pk_mul_f32 v[78:79], v[78:79], v[30:31] op_sel_hi:[1,0]\n"
    "v_pk_mul_f32 v[80:81], v[80:81], v[30:31] op_sel_hi:[1,0]\n"
    "v_pk_mul_f32 v[82:83], v[82:83], v[30:31] op_sel_hi:[1,0]\n"
    "v_pk_mul_f32 v[84:85], v[84:85], v[30:31] op_sel_hi:[1,0]\n"
    "v_pk_mul_f32 v[86:87], v[86:87], v[30:31] op_sel_hi:[1,0]\n"
    "v_pk_mul_f32 v[88:89], v[88:89], v[30:31] op_sel_hi:[1,0]\n"
    "v_pk_mul_f32 v[90:91], v[90:91], v[30:31] op_sel_hi:[1,0]\n"
    "v_pk_mul_f32 v[92:93], v[92:93], v[30:31] op_sel_hi:[1,0]\n"
    "v_pk_mul_f32 v[94:95], v[94:95], v[30:31] op_sel_hi:[1,0]\n"
    "v_pk_mul_f32 v[96:97], v[96:97], v[30:31] op_sel_hi:[1,0]\n"
    "v_pk_mul_f32 v[98:99], v[98:99], v[30:31] op_sel_hi:[1,0]\n"
    "v_pk_mul_f32 v[100:101], v[100:101], v[30:31] op_sel_hi:[1,0]\n"
    "v_pk_mul_f32 v[102:103], v[102:103], v[30:31] op_sel_hi:[1,0]\n"
    "v_pk_mul_f32 v[104:105], v[104:105], v[30:31] op_sel_hi:[1,0]\n"
    "v_pk_mul_f32 v[106:107], v[106:107], v[30:31] op_sel_hi:[1,0]\n"
    "v_pk_mul_f32 v[108:109], v[108:109], v[30:31] op_sel_hi:[1,0]\n"
    "v_pk_mul_f32 v[110:111], v[110:111], v[30:31] op_sel_hi:[1,0]\n"
    "v_pk_mul_f32 v[112:113], v[112:113], v[30:31] op_sel_hi:[1,0]\n"
    "v_pk_mul_f32 v[114:115], v[114:115], v[30:31] op_sel_hi:[1,0]\n"
    "v_pk_mul_f32 v[116:117], v[116:117], v[30:31] op_sel_hi:[1,0]\n"
    "v_pk_mul_f32 v[118:119], v[118:119], v[30:31] op_sel_hi:[1,0]\n"
    "v_pk_mul_f32 v[120:121], v[120:121], v[30:31] op_sel_hi:[1,0]\n"
    "v_pk_mul_f32 v[122:123], v[122:123], v[30:31] op_sel_hi:[1,0]\n"
    "v_pk_mul_f32 v[124:125], v[124:125], v[30:31] op_sel_hi:[1,0]\n"
    "v_pk_mul_f32 v[126:127], v[126:127], v[30:31] op_sel_hi:[1,0]\n"
    "v_mul_f32 v25, v25, v30\n"
    "v_exp_f32 v128, v128\n"
    "v_exp_f32 v129, v129\n"
    "v_exp_f32 v130, v130\n"
    "v_exp_f32 v131, v131\n"
    "v_exp_f32 v132, v132\n"
    "v_add_f32 v26, v128, v130\n"
    "v_exp_f32 v133, v133\n"
    "v_add_f32 v28, v129, v131\n"
    "v_cvt_pk_bf16_f32 v128, v128, v129\n"
    "v_cvt_pk_bf16_f32 v129, v130, v131\n"
    "v_exp_f32 v134, v134\n"
    "v_add_f32 v26, v26, v132\n"
    "v_exp_f32 v135, v135\n"
    "v_add_f32 v28, v28, v133\n"
    "v_cvt_pk_bf16_f32 v130, v132, v133\n"
    "v_exp_f32 v136, v136\n"
    "v_add_f32 v26, v26, v134\n"
    "v_exp_f32 v137, v137\n"
    "v_add_f32 v28, v28, v135\n"
    "v_cvt_pk_bf16_f32 v131, v134, v135\n"
    "v_exp_f32 v138, v138\n"
    "v_add_f32 v26, v26, v136\n"
    "v_exp_f32 v139, v139\n"
    "v_add_f32 v28, v28, v137\n"
    "v_cvt_pk_bf16_f32 v136, v136, v137\n"
    "v_exp_f32 v140, v140\n"
    "v_add_f32 v26, v26, v138\n"
    "v_exp_f32 v141, v141\n"
    "v_add_f32 v28, v28, v139\n"
    "v_cvt_pk_bf16_f32 v137, v138, v139\n"
    "v_exp_f32 v142, v142\n"
    "v_add_f32 v26, v26, v140\n"
    "v_exp_f32 v143, v143\n"
    "v_add_f32 v28, v28, v141\n"
    "v_cvt_pk_bf16_f32 v138, v140, v141\n"
    "v_add_f32 v26, v26, v142\n"
    "v_add_f32 v28, v28, v143\n"
    "v_cvt_pk_bf16_f32 v139, v142, v143\n"
    "v_exp_f32 v144, v144\n"
    "v_exp_f32 v145, v145\n"
    "v_exp_f32 v146, v146\n"
    "v_add_f32 v26, v26, v144\n"
    "v_exp_f32 v147, v147\n"
    "v_add_f32 v28, v28, v145\n"
    "v_cvt_pk_bf16_f32 v144, v144, v145\n"
    "v_exp_f32 v148, v148\n"
    "v_add_f32 v26, v26, v146\n"
    "v_exp_f32 v149, v149\n"
    "v_add_f32 v28, v28, v147\n"
    "v_cvt_pk_bf16_f32 v145, v146, v147\n"
    "v_exp_f32 v150, v150\n"
    "v_add_f32 v26, v26, v148\n"
    "v_exp_f32 v151, v151\n"
    "v_add_f32 v28, v28, v149\n"
    "v_cvt_pk_bf16_f32 v146, v148, v149\n"
    "v_exp_f32 v152, v152\n"
    "v_add_f32 v26, v26, v150\n"
    "v_exp_f32 v153, v153\n"
    "v_add_f32 v28, v28, v151\n"
    "v_cvt_pk_bf16_f32 v147, v150, v151\n"
    "v_exp_f32 v154, v154\n"
    "v_add_f32 v26, v26, v152\n"
    "v_exp_f32 v155, v155\n"
    "v_add_f32 v28, v28, v153\n"
    "v_cvt_pk_bf16_f32 v152, v152, v153\n"
    "v_exp_f32 v156, v156\n"
    "v_add_f32 v26, v26, v154\n"
    "v_exp_f32 v157, v157\n"
    "v_add_f32 v28, v28, v155\n"
    "v_cvt_pk_bf16_f32 v153, v154, v155\n"
    "v_exp_f32 v158, v158\n"
    "v_add_f32 v26, v26, v156\n"
    "v_exp_f32 v159, v159\n"
    "v_add_f32 v28, v28, v157\n"
    "v_cvt_pk_bf16_f32 v154, v156, v157\n"
    "v_add_f32 v26, v26, v158\n"
    "v_add_f32 v28, v28, v159\n"
    "v_cvt_pk_bf16_f32 v155, v158, v159\n"
    "v_add_f32 v26, v26, v28\n"
    "s_nop 1\n"
    "s_branch Lrxar_%=\n"
    "Lrxb_%=:\n"
    "v_and_b32 v1, 31, v0\n"
    "v_lshlrev_b32 v1, 8, v1\n"
    "v_lshrrev_b32 v2, 5, v0\n"
    "v_lshl_add_u32 v1, v2, 7, v1\n"
    "s_lshl_b32 s78, s65, 4\n"
    "s_add_u32 s78, s78, s64\n"
    "s_lshl_b32 s78, s78, 17\n"
    "s_lshl_b32 s79, s87, 20\n"
    "s_add_u32 s78, s78, s79\n"
    "s_lshl_b32 s79, s69, 14\n"
    "s_add_u32 s79, s79, s35\n"
    "s_and_b32 s79, s79, 0x1c000\n"
    "s_add_u32 s78, s78, s79\n"
    "s_nop 7\n"
    "s_waitcnt lgkmcnt(0)\n"
    "buffer_load_dwordx4 a[0:3], v1, s[40:43], s78 offen offset:0\n"
    "buffer_load_dwordx4 a[4:7], v1, s[40:43], s78 offen offset:16\n"
    "buffer_load_dwordx4 a[8:11], v1, s[40:43], s78 offen offset:32\n"
    "buffer_load_dwordx4 a[12:15], v1, s[40:43], s78 offen offset:48\n"
    "buffer_load_dwordx4 a[16:19], v1, s[40:43], s78 offen offset:64\n"
    "buffer_load_dwordx4 a[20:23], v1, s[40:43], s78 offen offset:80\n"
    "buffer_load_dwordx4 a[24:27], v1, s[40:43], s78 offen offset:96\n"
    "buffer_load_dwordx4 a[28:31], v1, s[40:43], s78 offen offset:112\n"
    "s_add_u32 s78, s78, 8192\n"
    "buffer_load_dwordx4 a[32:35], v1, s[40:43], s78 offen offset:0\n"
    "buffer_load_dwordx4 a[36:39], v1, s[40:43], s78 offen offset:16\n"
    "buffer_load_dwordx4 a[40:43], v1, s[40:43], s78 offen offset:32\n"
    "buffer_load_dwordx4 a[44:47], v1, s[40:43], s78 offen offset:48\n"
    "buffer_load_dwordx4 a[48:51], v1, s[40:43], s78 offen offset:64\n"
    "buffer_load_dwordx4 a[52:55], v1, s[40:43], s78 offen offset:80\n"
    "buffer_load_dwordx4 a[56:59], v1, s[40:43], s78 offen offset:96\n"
    "buffer_load_dwordx4 a[60:63], v1, s[40:43], s78 offen offset:112\n"
    "s_waitcnt vmcnt(0)\n"
    "v_mfma_f32_32x32x16_bf16 v[224:239], a[0:3], v[32:35], 0\n"
    "v_mfma_f32_32x32x16_bf16 v[224:239], a[4:7], v[36:39], v[224:239]\n"
    "v_mfma_f32_32x32x16_bf16 v[224:239], a[8:11], v[40:43], v[224:239]\n"
    "v_mfma_f32_32x32x16_bf16 v[224:239], a[12:15], v[44:47], v[224:239]\n"
    "v_mfma_f32_32x32x16_bf16 v[224:239], a[16:19], v[48:51], v[224:239]\n"
    "v_mfma_f32_32x32x16_bf16 v[224:239], a[20:23], v[52:55], v[224:239]\n"
    "v_mfma_f32_32x32x16_bf16 v[224:239], a[24:27], v[56:59], v[224:239]\n"
    "v_mfma_f32_32x32x16_bf16 v[224:239], a[28:31], v[60:63], v[224:239]\n"
    "v_mfma_f32_32x32x16_bf16 v[240:255], a[32:35], v[32:35], 0\n"
    "v_mfma_f32_32x32x16_bf16 v[240:255], a[36:39], v[36:39], v[240:255]\n"
    "v_mfma_f32_32x32x16_bf16 v[240:255], a[40:43], v[40:43], v[240:255]\n"
    "v_mfma_f32_32x32x16_bf16 v[240:255], a[44:47], v[44:47], v[240:255]\n"
    "v_mfma_f32_32x32x16_bf16 v[240:255], a[48:51], v[48:51], v[240:255]\n"
    "v_mfma_f32_32x32x16_bf16 v[240:255], a[52:55], v[52:55], v[240:255]\n"
    "v_mfma_f32_32x32x16_bf16 v[240:255], a[56:59], v[56:59], v[240:255]\n"
    "v_mfma_f32_32x32x16_bf16 v[240:255], a[60:63], v[60:63], v[240:255]\n"
    "s_nop 3\n"
    "v_max3_f32 v26, v224, v225, v226\n"
    "v_max3_f32 v28, v227, v228, v229\n"
    "v_max3_f32 v26, v26, v230, v231\n"
    "v_max3_f32 v28, v28, v232, v233\n"
    "v_max3_f32 v26, v26, v234, v235\n"
    "v_max3_f32 v28, v28, v236, v237\n"
    "v_max3_f32 v26, v26, v238, v239\n"
    "s_nop 0\n"
    "v_max3_f32 v28, v28, v240, v241\n"
    "v_max3_f32 v26, v26, v242, v243\n"
    "v_max3_f32 v28, v28, v244, v245\n"
    "v_max3_f32 v26, v26, v246, v247\n"
    "v_max3_f32 v28, v28, v248, v249\n"
    "v_max3_f32 v26, v26, v250, v251\n"
    "v_max3_f32 v28, v28, v252, v253\n"
    "v_max3_f32 v26, v26, v254, v255\n"
    "v_max_f32 v26, v26, v28\n"
    "v_mov_b32 v28, v26\n"
    "s_nop 1\n"
    "v_permlane32_swap_b32 v26, v28\n"
    "v_max_f32 v26, v26, v28\n"
    "v_max_f32 v26, v26, -v29\n"
    "v_add_f32 v28, v26, v29\n"
    "v_sub_f32 v29, 0, v26\n"
    "v_exp_f32 v30, -v28\n"
    "v_sub_f32 v224, v224, v26\n"
    "v_sub_f32 v225, v225, v26\n"
    "v_sub_f32 v226, v226, v26\n"
    "v_sub_f32 v227, v227, v26\n"
    "v_sub_f32 v228, v228, v26\n"
    "v_sub_f32 v229, v229, v26\n"
    "v_sub_f32 v230, v230, v26\n"
    "v_sub_f32 v231, v231, v26\n"
    "v_sub_f32 v232, v232, v26\n"
    "v_sub_f32 v233, v233, v26\n"
    "v_sub_f32 v234, v234, v26\n"
    "v_sub_f32 v235, v235, v26\n"
    "v_sub_f32 v236, v236, v26\n"
    "v_sub_f32 v237, v237, v26\n"
    "v_sub_f32 v238, v238, v26\n"
    "v_sub_f32 v239, v239, v26\n"
    "v_sub_f32 v240, v240, v26\n"
    "v_sub_f32 v241, v241, v26\n"
    "v_sub_f32 v242, v242, v26\n"
    "v_sub_f32 v243, v243, v26\n"
    "v_sub_f32 v244, v244, v26\n"
    "v_sub_f32 v245, v245, v26\n"
    "v_sub_f32 v246, v246, v26\n"
    "v_sub_f32 v247, v247, v26\n"
    "v_sub_f32 v248, v248, v26\n"
    "v_sub_f32 v249, v249, v26\n"
    "v_sub_f32 v250, v250, v26\n"
    "v_sub_f32 v251, v251, v26\n"
    "v_sub_f32 v252, v252, v26\n"
    "v_sub_f32 v253, v253, v26\n"
    "v_sub_f32 v254, v254, v26\n"
    "v_sub_f32 v255, v255, v26\n"
    "v_sub_f32 v128, v128, v28\n"
    "v_sub_f32 v129, v129, v28\n"
    "v_sub_f32 v130, v130, v28\n"
    "v_sub_f32 v131, v131, v28\n"
    "v_sub_f32 v132, v132, v28\n"
    "v_sub_f32 v133, v133, v28\n"
    "v_sub_f32 v134, v134, v28\n"
    "v_sub_f32 v135, v135, v28\n"
    "v_sub_f32 v136, v136, v28\n"
    "v_sub_f32 v137, v137, v28\n"
    "v_sub_f32 v138, v138, v28\n"
    "v_sub_f32 v139, v139, v28\n"
    "v_sub_f32 v140, v140, v28\n"
    "v_sub_f32 v141, v141, v28\n"
    "v_sub_f32 v142, v142, v28\n"
    "v_sub_f32 v143, v143, v28\n"
    "v_sub_f32 v144, v144, v28\n"
    "v_sub_f32 v145, v145, v28\n"
    "v_sub_f32 v146, v146, v28\n"
    "v_sub_f32 v147, v147, v28\n"
    "v_sub_f32 v148, v148, v28\n"
    "v_sub_f32 v149, v149, v28\n"
    "v_sub_f32 v150, v150, v28\n"
    "v_sub_f32 v151, v151, v28\n"
    "v_sub_f32 v152, v152, v28\n"
    "v_sub_f32 v153, v153, v28\n"
    "v_sub_f32 v154, v154, v28\n"
    "v_sub_f32 v155, v155, v28\n"
    "v_sub_f32 v156, v156, v28\n"
    "v_sub_f32 v157, v157, v28\n"
    "v_sub_f32 v158, v158, v28\n"
    "v_sub_f32 v159, v159, v28\n"
    "v_mov_b32 v160, v29\n"
    "v_mov_b32 v161, v29\n"
    "v_mov_b32 v162, v29\n"
    "v_mov_b32 v163, v29\n"
    "v_mov_b32 v164, v29\n"
    "v_mov_b32 v165, v29\n"
    "v_mov_b32 v166, v29\n"
    "v_mov_b32 v167, v29\n"
    "v_mov_b32 v168, v29\n"
    "v_mov_b32 v169, v29\n"
    "v_mov_b32 v170, v29\n"
    "v_mov_b32 v171, v29\n"
    "v_mov_b32 v172, v29\n"
    "v_mov_b32 v173, v29\n"
    "v_mov_b32 v174, v29\n"
    "v_mov_b32 v175, v29\n"
    "v_pk_mul_f32 v[64:65], v[64:65], v[30:31] op_sel_hi:[1,0]\n"
    "v_pk_mul_f32 v[66:67], v[66:67], v[30:31] op_sel_hi:[1,0]\n"
    "v_pk_mul_f32 v[68:69], v[68:69], v[30:31] op_sel_hi:[1,0]\n"
    "v_pk_mul_f32 v[70:71], v[70:71], v[30:31] op_sel_hi:[1,0]\n"
    "v_pk_mul_f32 v[72:73], v[72:73], v[30:31] op_sel_hi:[1,0]\n"
    "v_pk_mul_f32 v[74:75], v[74:75], v[30:31] op_sel_hi:[1,0]\n"
    "v_pk_mul_f32 v[76:77], v[76:77], v[30:31] op_sel_hi:[1,0]\n"
    "v_pk_mul_f32 v[78:79], v[78:79], v[30:31] op_sel_hi:[1,0]\n"
    "v_pk_mul_f32 v[80:81], v[80:81], v[30:31] op_sel_hi:[1,0]\n"
    "v_pk_mul_f32 v[82:83], v[82:83], v[30:31] op_sel_hi:[1,0]\n"
    "v_pk_mul_f32 v[84:85], v[84:85], v[30:31] op_sel_hi:[1,0]\n"
    "v_pk_mul_f32 v[86:87], v[86:87], v[30:31] op_sel_hi:[1,0]\n"
    "v_pk_mul_f32 v[88:89], v[88:89], v[30:31] op_sel_hi:[1,0]\n"
    "v_pk_mul_f32 v[90:91], v[90:91], v[30:31] op_sel_hi:[1,0]\n"
    "v_pk_mul_f32 v[92:93], v[92:93], v[30:31] op_sel_hi:[1,0]\n"
    "v_pk_mul_f32 v[94:95], v[94:95], v[30:31] op_sel_hi:[1,0]\n"
    "v_pk_mul_f32 v[96:97], v[96:97], v[30:31] op_sel_hi:[1,0]\n"
    "v_pk_mul_f32 v[98:99], v[98:99], v[30:31] op_sel_hi:[1,0]\n"
    "v_pk_mul_f32 v[100:101], v[100:101], v[30:31] op_sel_hi:[1,0]\n"
    "v_pk_mul_f32 v[102:103], v[102:103], v[30:31] op_sel_hi:[1,0]\n"
    "v_pk_mul_f32 v[104:105], v[104:105], v[30:31] op_sel_hi:[1,0]\n"
    "v_pk_mul_f32 v[106:107], v[106:107], v[30:31] op_sel_hi:[1,0]\n"
    "v_pk_mul_f32 v[108:109], v[108:109], v[30:31] op_sel_hi:[1,0]\n"
    "v_pk_mul_f32 v[110:111], v[110:111], v[30:31] op_sel_hi:[1,0]\n"
    "v_pk_mul_f32 v[112:113], v[112:113], v[30:31] op_sel_hi:[1,0]\n"
    "v_pk_mul_f32 v[114:115], v[114:115], v[30:31] op_sel_hi:[1,0]\n"
    "v_pk_mul_f32 v[116:117], v[116:117], v[30:31] op_sel_hi:[1,0]\n"
    "v_pk_mul_f32 v[118:119], v[118:119], v[30:31] op_sel_hi:[1,0]\n"
    "v_pk_mul_f32 v[120:121], v[120:121], v[30:31] op_sel_hi:[1,0]\n"
    "v_pk_mul_f32 v[122:123], v[122:123], v[30:31] op_sel_hi:[1,0]\n"
    "v_pk_mul_f32 v[124:125], v[124:125], v[30:31] op_sel_hi:[1,0]\n"
    "v_pk_mul_f32 v[126:127], v[126:127], v[30:31] op_sel_hi:[1,0]\n"
    "v_mul_f32 v25, v25, v30\n"
    "v_exp_f32 v224, v224\n"
    "v_exp_f32 v225, v225\n"
    "v_exp_f32 v226, v226\n"
    "v_exp_f32 v227, v227\n"
    "v_exp_f32 v228, v228\n"
    "v_add_f32 v26, v224, v226\n"
    "v_exp_f32 v229, v229\n"
    "v_add_f32 v28, v225, v227\n"
    "v_cvt_pk_bf16_f32 v224, v224, v225\n"
    "v_cvt_pk_bf16_f32 v225, v226, v227\n"
    "v_exp_f32 v230, v230\n"
    "v_add_f32 v26, v26, v228\n"
    "v_exp_f32 v231, v231\n"
    "v_add_f32 v28, v28, v229\n"
    "v_cvt_pk_bf16_f32 v226, v228, v229\n"
    "v_exp_f32 v232, v232\n"
    "v_add_f32 v26, v26, v230\n"
    "v_exp_f32 v233, v233\n"
    "v_add_f32 v28, v28, v231\n"
    "v_cvt_pk_bf16_f32 v227, v230, v231\n"
    "v_exp_f32 v234, v234\n"
    "v_add_f32 v26, v26, v232\n"
    "v_exp_f32 v235, v235\n"
    "v_add_f32 v28, v28, v233\n"
    "v_cvt_pk_bf16_f32 v232, v232, v233\n"
    "v_exp_f32 v236, v236\n"
    "v_add_f32 v26, v26, v234\n"
    "v_exp_f32 v237, v237\n"
    "v_add_f32 v28, v28, v235\n"
    "v_cvt_pk_bf16_f32 v233, v234, v235\n"
    "v_exp_f32 v238, v238\n"
    "v_add_f32 v26, v26, v236\n"
    "v_exp_f32 v239, v239\n"
    "v_add_f32 v28, v28, v237\n"
    "v_cvt_pk_bf16_f32 v234, v236, v237\n"
    "v_add_f32 v26, v26, v238\n"
    "v_add_f32 v28, v28, v239\n"
    "v_cvt_pk_bf16_f32 v235, v238, v239\n"
    "v_exp_f32 v240, v240\n"
    "v_exp_f32 v241, v241\n"
    "v_exp_f32 v242, v242\n"
    "v_add_f32 v26, v26, v240\n"
    "v_exp_f32 v243, v243\n"
    "v_add_f32 v28, v28, v241\n"
    "v_cvt_pk_bf16_f32 v240, v240, v241\n"
    "v_exp_f32 v244, v244\n"
    "v_add_f32 v26, v26, v242\n"
    "v_exp_f32 v245, v245\n"
    "v_add_f32 v28, v28, v243\n"
    "v_cvt_pk_bf16_f32 v241, v242, v243\n"
    "v_exp_f32 v246, v246\n"
    "v_add_f32 v26, v26, v244\n"
    "v_exp_f32 v247, v247\n"
    "v_add_f32 v28, v28, v245\n"
    "v_cvt_pk_bf16_f32 v242, v244, v245\n"
    "v_exp_f32 v248, v248\n"
    "v_add_f32 v26, v26, v246\n"
    "v_exp_f32 v249, v249\n"
    "v_add_f32 v28, v28, v247\n"
    "v_cvt_pk_bf16_f32 v243, v246, v247\n"
    "v_exp_f32 v250, v250\n"
    "v_add_f32 v26, v26, v248\n"
    "v_exp_f32 v251, v251\n"
    "v_add_f32 v28, v28, v249\n"
    "v_cvt_pk_bf16_f32 v248, v248, v249\n"
    "v_exp_f32 v252, v252\n"
    "v_add_f32 v26, v26, v250\n"
    "v_exp_f32 v253, v253\n"
    "v_add_f32 v28, v28, v251\n"
    "v_cvt_pk_bf16_f32 v249, v250, v251\n"
    "v_exp_f32 v254, v254\n"
    "v_add_f32 v26, v26, v252\n"
    "v_exp_f32 v255, v255\n"
    "v_add_f32 v28, v28, v253\n"
    "v_cvt_pk_bf16_f32 v250, v252, v253\n"
    "v_add_f32 v26, v26, v254\n"
    "v_add_f32 v28, v28, v255\n"
    "v_cvt_pk_bf16_f32 v251, v254, v255\n"
    "v_add_f32 v26, v26, v28\n"
    "s_nop 1\n"
    "s_branch Lrxbr_%=\n"
    "Lrxl_%=:\n"
    "v_and_b32 v1, 31, v0\n"
    "v_lshlrev_b32 v1, 8, v1\n"
    "v_lshrrev_b32 v2, 5, v0\n"
    "v_lshl_add_u32 v1, v2, 7, v1\n"
    "s_lshl_b32 s78, s65, 4\n"
    "s_add_u32 s78, s78, s64\n"
    "s_lshl_b32 s78, s78, 17\n"
    "s_lshl_b32 s79, s87, 20\n"
    "s_add_u32 s78, s78, s79\n"
    "s_lshl_b32 s79, s69, 14\n"
    "s_add_u32 s79, s79, s35\n"
    "s_and_b32 s79, s79, 0x1c000\n"
    "s_add_u32 s78, s78, s79\n"
    "s_nop 7\n"
    "s_waitcnt lgkmcnt(0)\n"
    "buffer_load_dwordx4 a[0:3], v1, s[40:43], s78 offen offset:0\n"
    "buffer_load_dwordx4 a[4:7], v1, s[40:43], s78 offen offset:16\n"
    "buffer_load_dwordx4 a[8:11], v1, s[40:43], s78 offen offset:32\n"
    "buffer_load_dwordx4 a[12:15], v1, s[40:43], s78 offen offset:48\n"
    "buffer_load_dwordx4 a[16:19], v1, s[40:43], s78 offen offset:64\n"
    "buffer_load_dwordx4 a[20:23], v1, s[40:43], s78 offen offset:80\n"
    "buffer_load_dwordx4 a[24:27], v1, s[40:43], s78 offen offset:96\n"
    "buffer_load_dwordx4 a[28:31], v1, s[40:43], s78 offen offset:112\n"
    "s_add_u32 s78, s78, 8192\n"
    "buffer_load_dwordx4 a[32:35], v1, s[40:43], s78 offen offset:0\n"
    "buffer_load_dwordx4 a[36:39], v1, s[40:43], s78 offen offset:16\n"
    "buffer_load_dwordx4 a[40:43], v1, s[40:43], s78 offen offset:32\n"
    "buffer_load_dwordx4 a[44:47], v1, s[40:43], s78 offen offset:48\n"
    "buffer_load_dwordx4 a[48:51], v1, s[40:43], s78 offen offset:64\n"
    "buffer_load_dwordx4 a[52:55], v1, s[40:43], s78 offen offset:80\n"
    "buffer_load_dwordx4 a[56:59], v1, s[40:43], s78 offen offset:96\n"
    "buffer_load_dwordx4 a[60:63], v1, s[40:43], s78 offen offset:112\n"
    "s_waitcnt vmcnt(0)\n"
    "v_mfma_f32_32x32x16_bf16 v[224:239], a[0:3], v[32:35], 0\n"
    "v_mfma_f32_32x32x16_bf16 v[224:239], a[4:7], v[36:39], v[224:239]\n"
    "v_mfma_f32_32x32x16_bf16 v[224:239], a[8:11], v[40:43], v[224:239]\n"
    "v_mfma_f32_32x32x16_bf16 v[224:239], a[12:15], v[44:47], v[224:239]\n"
    "v_mfma_f32_32x32x16_bf16 v[224:239], a[16:19], v[48:51], v[224:239]\n"
    "v_mfma_f32_32x32x16_bf16 v[224:239], a[20:23], v[52:55], v[224:239]\n"
    "v_mfma_f32_32x32x16_bf16 v[224:239], a[24:27], v[56:59], v[224:239]\n"
    "v_mfma_f32_32x32x16_bf16 v[224:239], a[28:31], v[60:63], v[224:239]\n"
    "v_mfma_f32_32x32x16_bf16 v[240:255], a[32:35], v[32:35], 0\n"
    "v_mfma_f32_32x32x16_bf16 v[240:255], a[36:39], v[36:39], v[240:255]\n"
    "v_mfma_f32_32x32x16_bf16 v[240:255], a[40:43], v[40:43], v[240:255]\n"
    "v_mfma_f32_32x32x16_bf16 v[240:255], a[44:47], v[44:47], v[240:255]\n"
    "v_mfma_f32_32x32x16_bf16 v[240:255], a[48:51], v[48:51], v[240:255]\n"
    "v_mfma_f32_32x32x16_bf16 v[240:255], a[52:55], v[52:55], v[240:255]\n"
    "v_mfma_f32_32x32x16_bf16 v[240:255], a[56:59], v[56:59], v[240:255]\n"
    "v_mfma_f32_32x32x16_bf16 v[240:255], a[60:63], v[60:63], v[240:255]\n"
    "s_nop 3\n"
    "v_max3_f32 v26, v224, v225, v226\n"
    "v_max3_f32 v28, v227, v228, v229\n"
    "v_max3_f32 v26, v26, v230, v231\n"
    "v_max3_f32 v28, v28, v232, v233\n"
    "v_max3_f32 v26, v26, v234, v235\n"
    "v_max3_f32 v28, v28, v236, v237\n"
    "v_max3_f32 v26, v26, v238, v239\n"
    "s_nop 0\n"
    "v_max3_f32 v28, v28, v240, v241\n"
    "v_max3_f32 v26, v26, v242, v243\n"
    "v_max3_f32 v28, v28, v244, v245\n"
    "v_max3_f32 v26, v26, v246, v247\n"
    "v_max3_f32 v28, v28, v248, v249\n"
    "v_max3_f32 v26, v26, v250, v251\n"
    "v_max3_f32 v28, v28, v252, v253\n"
    "v_max3_f32 v26, v26, v254, v255\n"
    "v_max_f32 v26, v26, v28\n"
    "v_mov_b32 v28, v26\n"
    "s_nop 1\n"
    "v_permlane32_swap_b32 v26, v28\n"
    "v_max_f32 v26, v26, v28\n"
    "v_max_f32 v26, v26, -v29\n"
    "v_add_f32 v28, v26, v29\n"
    "v_sub_f32 v29, 0, v26\n"
    "v_exp_f32 v30, -v28\n"
    "v_sub_f32 v224, v224, v26\n"
    "v_sub_f32 v225, v225, v26\n"
    "v_sub_f32 v226, v226, v26\n"
    "v_sub_f32 v227, v227, v26\n"
    "v_sub_f32 v228, v228, v26\n"
    "v_sub_f32 v229, v229, v26\n"
    "v_sub_f32 v230, v230, v26\n"
    "v_sub_f32 v231, v231, v26\n"
    "v_sub_f32 v232, v232, v26\n"
    "v_sub_f32 v233, v233, v26\n"
    "v_sub_f32 v234, v234, v26\n"
    "v_sub_f32 v235, v235, v26\n"
    "v_sub_f32 v236, v236, v26\n"
    "v_sub_f32 v237, v237, v26\n"
    "v_sub_f32 v238, v238, v26\n"
    "v_sub_f32 v239, v239, v26\n"
    "v_sub_f32 v240, v240, v26\n"
    "v_sub_f32 v241, v241, v26\n"
    "v_sub_f32 v242, v242, v26\n"
    "v_sub_f32 v243, v243, v26\n"
    "v_sub_f32 v244, v244, v26\n"
    "v_sub_f32 v245, v245, v26\n"
    "v_sub_f32 v246, v246, v26\n"
    "v_sub_f32 v247, v247, v26\n"
    "v_sub_f32 v248, v248, v26\n"
    "v_sub_f32 v249, v249, v26\n"
    "v_sub_f32 v250, v250, v26\n"
    "v_sub_f32 v251, v251, v26\n"
    "v_sub_f32 v252, v252, v26\n"
    "v_sub_f32 v253, v253, v26\n"
    "v_sub_f32 v254, v254, v26\n"
    "v_sub_f32 v255, v255, v26\n"
    "v_pk_mul_f32 v[64:65], v[64:65], v[30:31] op_sel_hi:[1,0]\n"
    "v_pk_mul_f32 v[66:67], v[66:67], v[30:31] op_sel_hi:[1,0]\n"
    "v_pk_mul_f32 v[68:69], v[68:69], v[30:31] op_sel_hi:[1,0]\n"
    "v_pk_mul_f32 v[70:71], v[70:71], v[30:31] op_sel_hi:[1,0]\n"
    "v_pk_mul_f32 v[72:73], v[72:73], v[30:31] op_sel_hi:[1,0]\n"
    "v_pk_mul_f32 v[74:75], v[74:75], v[30:31] op_sel_hi:[1,0]\n"
    "v_pk_mul_f32 v[76:77], v[76:77], v[30:31] op_sel_hi:[1,0]\n"
    "v_pk_mul_f32 v[78:79], v[78:79], v[30:31] op_sel_hi:[1,0]\n"
    "v_pk_mul_f32 v[80:81], v[80:81], v[30:31] op_sel_hi:[1,0]\n"
    "v_pk_mul_f32 v[82:83], v[82:83], v[30:31] op_sel_hi:[1,0]\n"
    "v_pk_mul_f32 v[84:85], v[84:85], v[30:31] op_sel_hi:[1,0]\n"
    "v_pk_mul_f32 v[86:87], v[86:87], v[30:31] op_sel_hi:[1,0]\n"
    "v_pk_mul_f32 v[88:89], v[88:89], v[30:31] op_sel_hi:[1,0]\n"
    "v_pk_mul_f32 v[90:91], v[90:91], v[30:31] op_sel_hi:[1,0]\n"
    "v_pk_mul_f32 v[92:93], v[92:93], v[30:31] op_sel_hi:[1,0]\n"
    "v_pk_mul_f32 v[94:95], v[94:95], v[30:31] op_sel_hi:[1,0]\n"
    "v_pk_mul_f32 v[96:97], v[96:97], v[30:31] op_sel_hi:[1,0]\n"
    "v_pk_mul_f32 v[98:99], v[98:99], v[30:31] op_sel_hi:[1,0]\n"
    "v_pk_mul_f32 v[100:101], v[100:101], v[30:31] op_sel_hi:[1,0]\n"
    "v_pk_mul_f32 v[102:103], v[102:103], v[30:31] op_sel_hi:[1,0]\n"
    "v_pk_mul_f32 v[104:105], v[104:105], v[30:31] op_sel_hi:[1,0]\n"
    "v_pk_mul_f32 v[106:107], v[106:107], v[30:31] op_sel_hi:[1,0]\n"
    "v_pk_mul_f32 v[108:109], v[108:109], v[30:31] op_sel_hi:[1,0]\n"
    "v_pk_mul_f32 v[110:111], v[110:111], v[30:31] op_sel_hi:[1,0]\n"
    "v_pk_mul_f32 v[112:113], v[112:113], v[30:31] op_sel_hi:[1,0]\n"
    "v_pk_mul_f32 v[114:115], v[114:115], v[30:31] op_sel_hi:[1,0]\n"
    "v_pk_mul_f32 v[116:117], v[116:117], v[30:31] op_sel_hi:[1,0]\n"
    "v_pk_mul_f32 v[118:119], v[118:119], v[30:31] op_sel_hi:[1,0]\n"
    "v_pk_mul_f32 v[120:121], v[120:121], v[30:31] op_sel_hi:[1,0]\n"
    "v_pk_mul_f32 v[122:123], v[122:123], v[30:31] op_sel_hi:[1,0]\n"
    "v_pk_mul_f32 v[124:125], v[124:125], v[30:31] op_sel_hi:[1,0]\n"
    "v_pk_mul_f32 v[126:127], v[126:127], v[30:31] op_sel_hi:[1,0]\n"
    "v_mul_f32 v25, v25, v30\n"
    "v_exp_f32 v224, v224\n"
    "v_exp_f32 v225, v225\n"
    "v_exp_f32 v226, v226\n"
    "v_exp_f32 v227, v227\n"
    "v_exp_f32 v228, v228\n"
    "v_add_f32 v26, v224, v226\n"
    "v_exp_f32 v229, v229\n"
    "v_add_f32 v28, v225, v227\n"
    "v_cvt_pk_bf16_f32 v224, v224, v225\n"
    "v_cvt_pk_bf16_f32 v225, v226, v227\n"
    "v_exp_f32 v230, v230\n"
    "v_add_f32 v26, v26, v228\n"
    "v_exp_f32 v231, v231\n"
    "v_add_f32 v28, v28, v229\n"
    "v_cvt_pk_bf16_f32 v226, v228, v229\n"
    "v_exp_f32 v232, v232\n"
    "v_add_f32 v26, v26, v230\n"
    "v_exp_f32 v233, v233\n"
    "v_add_f32 v28, v28, v231\n"
    "v_cvt_pk_bf16_f32 v227, v230, v231\n"
    "v_exp_f32 v234, v234\n"
    "v_add_f32 v26, v26, v232\n"
    "v_exp_f32 v235, v235\n"
    "v_add_f32 v28, v28, v233\n"
    "v_cvt_pk_bf16_f32 v232, v232, v233\n"
    "v_exp_f32 v236, v236\n"
    "v_add_f32 v26, v26, v234\n"
    "v_exp_f32 v237, v237\n"
    "v_add_f32 v28, v28, v235\n"
    "v_cvt_pk_bf16_f32 v233, v234, v235\n"
    "v_exp_f32 v238, v238\n"
    "v_add_f32 v26, v26, v236\n"
    "v_exp_f32 v239, v239\n"
    "v_add_f32 v28, v28, v237\n"
    "v_cvt_pk_bf16_f32 v234, v236, v237\n"
    "v_add_f32 v26, v26, v238\n"
    "v_add_f32 v28, v28, v239\n"
    "v_cvt_pk_bf16_f32 v235, v238, v239\n"
    "v_exp_f32 v240, v240\n"
    "v_exp_f32 v241, v241\n"
    "v_exp_f32 v242, v242\n"
    "v_add_f32 v26, v26, v240\n"
    "v_exp_f32 v243, v243\n"
    "v_add_f32 v28, v28, v241\n"
    "v_cvt_pk_bf16_f32 v240, v240, v241\n"
    "v_exp_f32 v244, v244\n"
    "v_add_f32 v26, v26, v242\n"
    "v_exp_f32 v245, v245\n"
    "v_add_f32 v28, v28, v243\n"
    "v_cvt_pk_bf16_f32 v241, v242, v243\n"
    "v_exp_f32 v246, v246\n"
    "v_add_f32 v26, v26, v244\n"
    "v_exp_f32 v247, v247\n"
    "v_add_f32 v28, v28, v245\n"
    "v_cvt_pk_bf16_f32 v242, v244, v245\n"
    "v_exp_f32 v248, v248\n"
    "v_add_f32 v26, v26, v246\n"
    "v_exp_f32 v249, v249\n"
    "v_add_f32 v28, v28, v247\n"
    "v_cvt_pk_bf16_f32 v243, v246, v247\n"
    "v_exp_f32 v250, v250\n"
    "v_add_f32 v26, v26, v248\n"
    "v_exp_f32 v251, v251\n"
    "v_add_f32 v28, v28, v249\n"
    "v_cvt_pk_bf16_f32 v248, v248, v249\n"
    "v_exp_f32 v252, v252\n"
    "v_add_f32 v26, v26, v250\n"
    "v_exp_f32 v253, v253\n"
    "v_add_f32 v28, v28, v251\n"
    "v_cvt_pk_bf16_f32 v249, v250, v251\n"
    "v_exp_f32 v254, v254\n"
    "v_add_f32 v26, v26, v252\n"
    "v_exp_f32 v255, v255\n"
    "v_add_f32 v28, v28, v253\n"
    "v_cvt_pk_bf16_f32 v250, v252, v253\n"
    "v_add_f32 v26, v26, v254\n"
    "v_add_f32 v28, v28, v255\n"
    "v_cvt_pk_bf16_f32 v251, v254, v255\n"
    "v_add_f32 v26, v26, v28\n"
    "s_nop 1\n"
    "s_setpc_b64 s[80:81]\n"
    "Lend_%=:\n"
    :
    : "s"(ka), "i"(0), "i"(0), "i"(0), "i"(0), "i"(0), "i"(0), "s"(bid), "s"(w)
    : "v0", "v1", "v2", "v3", "v4", "v5", "v6", "v7", "v8", "v9", "v10", "v11", "v12", "v13", "v14", "v15", "v16", "v17", "v18", "v19", "v20", "v21", "v22", "v23", "v24", "v25", "v26", "v27", "v28", "v29", "v30", "v31", "v32", "v33", "v34", "v35", "v36", "v37", "v38", "v39", "v40", "v41", "v42", "v43", "v44", "v45", "v46", "v47", "v48", "v49", "v50", "v51", "v52", "v53", "v54", "v55", "v56", "v57", "v58", "v59", "v60", "v61", "v62", "v63", "v64", "v65", "v66", "v67", "v68", "v69", "v70", "v71", "v72", "v73", "v74", "v75", "v76", "v77", "v78", "v79", "v80", "v81", "v82", "v83", "v84", "v85", "v86", "v87", "v88", "v89", "v90", "v91", "v92", "v93", "v94", "v95", "v96", "v97", "v98", "v99", "v100", "v101", "v102", "v103", "v104", "v105", "v106", "v107", "v108", "v109", "v110", "v111", "v112", "v113", "v114", "v115", "v116", "v117", "v118", "v119", "v120", "v121", "v122", "v123", "v124", "v125", "v126", "v127", "v128", "v129", "v130", "v131", "v132", "v133", "v134", "v135", "v136", "v137", "v138", "v139", "v140", "v141", "v142", "v143", "v144", "v145", "v146", "v147", "v148", "v149", "v150", "v151", "v152", "v153", "v154", "v155", "v156", "v157", "v158", "v159", "v160", "v161", "v162", "v163", "v164", "v165", "v166", "v167", "v168", "v169", "v170", "v171", "v172", "v173", "v174", "v175", "v176", "v177", "v178", "v179", "v180", "v181", "v182", "v183", "v184", "v185", "v186", "v187", "v188", "v189", "v190", "v191", "v192", "v193", "v194", "v195", "v196", "v197", "v198", "v199", "v200", "v201", "v202", "v203", "v204", "v205", "v206", "v207", "v208", "v209", "v210", "v211", "v212", "v213", "v214", "v215", "v216", "v217", "v218", "v219", "v220", "v221", "v222", "v223", "v224", "v225", "v226", "v227", "v228", "v229", "v230", "v231", "v232", "v233", "v234", "v235", "v236", "v237", "v238", "v239", "v240", "v241", "v242", "v243", "v244", "v245", "v246", "v247", "v248", "v249", "v250", "v251", "v252", "v253", "v254", "v255", "a0", "a1", "a2", "a3", "a4", "a5", "a6", "a7", "a8", "a9", "a10", "a11", "a12", "a13", "a14", "a15", "a16", "a17", "a18", "a19", "a20", "a21", "a22", "a23", "a24", "a25", "a26", "a27", "a28", "a29", "a30", "a31", "a32", "a33", "a34", "a35", "a36", "a37", "a38", "a39", "a40", "a41", "a42", "a43", "a44", "a45", "a46", "a47", "a48", "a49", "a50", "a51", "a52", "a53", "a54", "a55", "a56", "a57", "a58", "a59", "a60", "a61", "a62", "a63", "a64", "a65", "a66", "a67", "a68", "a69", "a70", "a71", "a72", "a73", "a74", "a75", "a76", "a77", "a78", "a79", "a80", "a81", "a82", "a83", "a84", "a85", "a86", "a87", "a88", "a89", "a90", "a91", "a92", "a93", "a94", "a95", "a96", "a97", "a98", "a99", "a100", "a101", "a102", "a103", "a104", "a105", "a106", "a107", "a108", "a109", "a110", "a111", "a112", "a113", "a114", "a115", "a116", "a117", "a118", "a119", "a120", "a121", "a122", "a123", "a124", "a125", "a126", "a127", "a128", "a129", "a130", "a131", "a132", "a133", "a134", "a135", "a136", "a137", "a138", "a139", "a140", "a141", "a142", "a143", "a144", "a145", "a146", "a147", "a148", "a149", "a150", "a151", "a152", "a153", "a154", "a155", "a156", "a157", "a158", "a159", "a160", "a161", "a162", "a163", "a164", "a165", "a166", "a167", "a168", "a169", "a170", "a171", "a172", "a173", "a174", "a175", "a176", "a177", "a178", "a179", "a180", "a181", "a182", "a183", "a184", "a185", "a186", "a187", "a188", "a189", "a190", "a191", "a192", "a193", "a194", "a195", "a196", "a197", "a198", "a199", "a200", "a201", "a202", "a203", "a204", "a205", "a206", "a207", "a208", "a209", "a210", "a211", "a212", "a213", "a214", "a215", "a216", "a217", "a218", "a219", "a220", "a221", "a222", "a223", "a224", "a225", "a226", "a227", "a228", "a229", "a230", "a231", "a232", "a233", "a234", "a235", "a236", "a237", "a238", "a239", "a240", "a241", "a242", "a243", "a244", "a245", "a246", "a247", "a248", "a249", "a250", "a251", "a252", "a253", "a254", "a255", "s33", "s34", "s35", "s36", "s37", "s38", "s39", "s40", "s41", "s42", "s43", "s44", "s45", "s46", "s47", "s48", "s49", "s50", "s51", "s52", "s53", "s54", "s55", "s56", "s57", "s58", "s59", "s60", "s61", "s62", "s63", "s64", "s65", "s66", "s67", "s68", "s69", "s70", "s71", "s72", "s73", "s74", "s75", "s76", "s77", "s78", "s79", "s80", "s81", "s82", "s83", "s84", "s85", "s86", "s87", "s88", "s89", "s90", "s91", "s92", "s93", "s94", "s95", "s96", "s97", "s98", "s99", "vcc", "scc", "memory");
}

extern "C" void kernel_launch(void* const* d_in, const int* in_sizes, int n_in, void* d_out, int out_size,
                              void* d_ws, size_t ws_size, hipStream_t stream) {
  (void)hipFuncSetAttribute((const void*)gqa_block_fused, hipFuncAttributeMaxDynamicSharedMemorySize, 163840);
  gqa_block_fused<<<256, 256, 163840, stream>>>(d_in[0], d_in[1], d_in[2], d_in[3], d_in[4], d_in[5], d_in[6], d_in[7],
                                                d_in[8], d_in[9], d_out, d_ws);
}
```
